```python
import torch

def _softmax(x, axis=-1):
    return torch.softmax(x, dim=axis)
def _gelu(x):
    return torch.nn.functional.gelu(x, approximate="tanh")
def _silu(x):
    return torch.nn.functional.silu(x)
def _sigmoid(x):
    return torch.sigmoid(x)
def _layernorm(x, axis=-1, eps=1e-5):
    m = x.mean(dim=axis, keepdim=True)
    v = x.var(dim=axis, keepdim=True, unbiased=False)
    return (x - m) / torch.sqrt(v + eps)
def _scatter_add(tgt, idx, src, axis=0):
    return tgt.clone().index_add_(axis, idx, src)
def _segment_sum(src, seg, n):
    out = torch.zeros((n,) + src.shape[1:], dtype=src.dtype)
    return out.index_add_(0, seg, src)
def _pad(x, pw):
    flat = tuple(v for pair in reversed(pw) for v in pair)
    return torch.nn.functional.pad(x, flat)

kernel_name = 'interleave_layers__ssm_conv_h3__sliding_block__prompt'

def setup_inputs():

    _w = (lambda a: (lambda t: t.to(torch.bfloat16) if t.is_floating_point() else t)(torch.as_tensor(a)))
    def _A_setup():
        B, S, D = 1, 2048, 448
        ks = 4
        return {
            "q": _w((lambda *s: torch.randn(*s, dtype=torch.bfloat16) * 0.05)(B, S, D)),
            "k": _w((lambda *s: torch.randn(*s, dtype=torch.bfloat16) * 0.4)(B, S, D)),
            "v": _w((lambda *s: torch.randn(*s, dtype=torch.bfloat16) * 0.4)(B, S, D)),
            "shift_k": _w((lambda *s: torch.randn(*s, dtype=torch.bfloat16) * 0.4)(D, ks) * 0.5),
            "ssm_k": _w((lambda *s: torch.randn(*s, dtype=torch.bfloat16) * 0.4)(D, S) * (1.0 / S ** 0.5)),
            "ssm_D": _w((lambda *s: torch.randn(*s, dtype=torch.bfloat16) * 0.4)(D)),
        }
    _A_in = _A_setup()

    def _B_setup():
        B, S, D = 1, 2048, 448
        return {
            "x": _w((lambda *s: torch.randn(*s, dtype=torch.bfloat16) * 0.05)(B, S, D)),
            "wqkv": _w((lambda *s: torch.randn(*s, dtype=torch.bfloat16) * 0.05)(D, 3 * D)),
            "wo": _w((lambda *s: torch.randn(*s, dtype=torch.bfloat16) * 0.05)(D, D)),
        }
    _B_in = _B_setup()

    return {
        "A_q": _A_in["q"],
        "A_k": _A_in["k"],
        "A_v": _A_in["v"],
        "A_shift_k": _A_in["shift_k"],
        "A_ssm_k": _A_in["ssm_k"],
        "A_ssm_D": _A_in["ssm_D"],
        "B_x": _B_in["x"],
        "B_wqkv": _B_in["wqkv"],
        "B_wo": _B_in["wo"],
    }

def reference(A_q, A_k, A_v, A_shift_k, A_ssm_k, A_ssm_D, B_x, B_wqkv, B_wo):
    def _A_ref(q, k, v, shift_k, ssm_k, ssm_D):
        B, S, D = q.shape
        ks = shift_k.shape[-1]
        k_pad = _pad(k, ((0, 0), (ks - 1, 0), (0, 0)))
        k_shift = torch.zeros_like(k)
        for t in range(ks):
            k_shift = k_shift + k_pad[:, t:t + S, :] * shift_k[:, ks - 1 - t][None, None, :]
        kv = k_shift * v
        L = 2 * S
        kv_pad = _pad(kv, ((0, 0), (0, S), (0, 0)))
        kf_pad = _pad(ssm_k, ((0, 0), (0, S)))
        kv_f = (lambda x, axes=(-2,-1): torch.fft.rfft2(x.float(), dim=axes))(kv_pad[:, None, :, :], axes=(1, 2))
        kf_f = (lambda x, axes=(-2,-1): torch.fft.rfft2(x.float(), dim=axes))(kf_pad[:, None, :], axes=(1, 2))
        kf_f = (lambda x, *d: x.permute(*d))(kf_f, 1, 2, 0)
        y_f = kv_f * kf_f[None, :, :, :]
        y = (lambda x, s, axes=(-2,-1): torch.fft.irfft2(x, s=s, dim=axes).to(torch.bfloat16))(y_f, (1, L), axes=(1, 2))[:, 0, :S, :]
        y = y + kv * ssm_D[None, None, :]
        return q * y

    def _B_ref(x, wqkv, wo):
        B, S, D = x.shape
        H = 16; Dh = D // H; W = 256
        qkv = (lambda x, *d: x.permute(*d))((x @ wqkv).reshape(B, S, 3, H, Dh), 2, 0, 3, 1, 4)
        q, k, v = qkv[0], qkv[1], qkv[2]
        scores = torch.einsum("bhqd,bhkd->bhqk", q, k) * (Dh ** -0.5)
        idx = torch.arange(S)
        mask = (idx[:, None] - idx[None, :] >= 0) & (idx[:, None] - idx[None, :] < W)
        scores = torch.where(mask[None, None], scores, -1e9)
        p = _softmax(scores, axis=-1)
        o = torch.einsum("bhqk,bhkd->bhqd", p, v)
        return (lambda x, *d: x.permute(*d))(o, 0, 2, 1, 3).reshape(B, S, D) @ wo

    x = A_q
    x = x + _A_ref(_layernorm(x, axis=-1), A_k, A_v, A_shift_k, A_ssm_k, A_ssm_D)
    x = x + _B_ref(_layernorm(x, axis=-1), B_wqkv, B_wo)
    x = x + _A_ref(_layernorm(x, axis=-1), A_k, A_v, A_shift_k, A_ssm_k, A_ssm_D)
    x = x + _B_ref(_layernorm(x, axis=-1), B_wqkv, B_wo)
    return x
```

```cpp
#include <hip/hip_runtime.h>
#include <hip/hip_bf16.h>
#include <stdint.h>

namespace nv {
constexpr int S = 2048, D = 448, H = 16, DH = 28, W = 256;
typedef unsigned short u16;
__device__ __forceinline__ float b2f(u16 b) { return __uint_as_float(((unsigned)b) << 16); }
__device__ __forceinline__ u16 f2b(float f) { __hip_bfloat16 h = __float2bfloat16(f); return *(u16*)&h; }
__device__ __forceinline__ float rb(float f) { return b2f(f2b(f)); }

__global__ void k_kv(const u16* k, const u16* v, const u16* sk, u16* kv) {
    int i = blockIdx.x * blockDim.x + threadIdx.x;
    if (i >= S * D) return;
    int t = i / D, c = i % D;
    float acc = 0.f;
    for (int j = 0; j < 4; ++j) {
        int tt = t + j - 3;
        float kk = tt >= 0 ? b2f(k[tt * D + c]) : 0.f;
        acc = rb(acc + rb(kk * b2f(sk[c * 4 + 3 - j])));
    }
    kv[i] = f2b(acc * b2f(v[i]));
}
__global__ void k_conv(const u16* kv, const u16* hk, const u16* sd, u16* Y) {
    int i = blockIdx.x * blockDim.x + threadIdx.x;
    if (i >= S * D) return;
    int t = i / D, c = i % D;
    float acc = 0.f;
    for (int m = 0; m <= t; ++m) acc += b2f(kv[m * D + c]) * b2f(hk[c * S + (t - m)]);
    float y = rb(acc);
    y = rb(y + rb(b2f(kv[i]) * b2f(sd[c])));
    Y[i] = f2b(y);
}
__device__ void ln_row(const u16* x, float* o  , int lane) {
    float v[7], s = 0.f;
    for (int j = 0; j < 7; ++j) { v[j] = b2f(x[lane + 64 * j]); s += v[j]; }
    for (int o2 = 32; o2; o2 >>= 1) s += __shfl_xor(s, o2);
    float m = s / D, q = 0.f;
    for (int j = 0; j < 7; ++j) { float d = v[j] - m; q += d * d; }
    for (int o2 = 32; o2; o2 >>= 1) q += __shfl_xor(q, o2);
    float var = rb(q / D); float mb = rb(m);
    float den = rb(sqrtf(rb(var + 1e-5f)));
    for (int j = 0; j < 7; ++j) o[j] = rb(rb(v[j] - mb) / den);
}
__global__ void k_alayer(const u16* x, const u16* Y, u16* xo) {
    int t = blockIdx.x, lane = threadIdx.x;
    float n[7]; ln_row(x + t * D, n, lane);
    for (int j = 0; j < 7; ++j) { int c = lane + 64 * j; xo[t * D + c] = f2b(b2f(x[t * D + c]) + rb(n[j] * b2f(Y[t * D + c]))); }
}
__global__ void k_ln(const u16* x, u16* h) {
    int t = blockIdx.x, lane = threadIdx.x;
    float n[7]; ln_row(x + t * D, n, lane);
    for (int j = 0; j < 7; ++j) { int c = lane + 64 * j; h[t * D + c] = f2b(n[j]); }
}
__global__ void k_gemm(const u16* A, const u16* B, const u16* res, u16* C, int M, int N, int K) {
    int i = blockIdx.x * blockDim.x + threadIdx.x;
    if (i >= M * N) return;
    int m = i / N, n = i % N;
    float acc = 0.f;
    for (int k = 0; k < K; ++k) acc += b2f(A[m * K + k]) * b2f(B[k * N + n]);
    float r = rb(acc);
    if (res) r = b2f(res[i]) + r;
    C[i] = f2b(r);
}
__global__ void k_attn(const u16* qkv, u16* o) {
    int t = blockIdx.x / H, h = blockIdx.x % H, j = threadIdx.x;
    __shared__ float p[256]; __shared__ float red[256];
    int kt = t - j;
    float s = -1e9f;
    const float scale = rb(0.18898223650461362f);
    if (kt >= 0) {
        float acc = 0.f;
        for (int d = 0; d < DH; ++d) acc += b2f(qkv[t * 3 * D + h * DH + d]) * b2f(qkv[kt * 3 * D + D + h * DH + d]);
        s = rb(rb(acc) * 0.18898223650461362f);
    }
    (void)scale;
    red[j] = s; __syncthreads();
    for (int o2 = 128; o2; o2 >>= 1) { if (j < o2) red[j] = fmaxf(red[j], red[j + o2]); __syncthreads(); }
    float mx = red[0]; __syncthreads();
    float e = kt >= 0 ? __expf(s - mx) : 0.f;
    red[j] = e; __syncthreads();
    for (int o2 = 128; o2; o2 >>= 1) { if (j < o2) red[j] += red[j + o2]; __syncthreads(); }
    float sum = red[0];
    p[j] = rb(e / sum); __syncthreads();
    if (j < DH) {
        float acc = 0.f;
        for (int jj = 0; jj < 256 && t - jj >= 0; ++jj) acc += p[jj] * b2f(qkv[(t - jj) * 3 * D + 2 * D + h * DH + j]);
        o[t * D + h * DH + j] = f2b(acc);
    }
}
}

extern "C" void kernel_launch(void* const* d_in, const int* in_sizes, int n_in,
                              void* d_out, int out_size, void* d_ws, size_t ws_size,
                              hipStream_t stream) {
    using namespace nv;
    const u16* q = (const u16*)d_in[0]; const u16* k = (const u16*)d_in[1]; const u16* v = (const u16*)d_in[2];
    const u16* sk = (const u16*)d_in[3]; const u16* hk = (const u16*)d_in[4]; const u16* sd = (const u16*)d_in[5];
    const u16* wqkv = (const u16*)d_in[7]; const u16* wo = (const u16*)d_in[8];
    u16* out = (u16*)d_out;
    u16* ws = (u16*)d_ws;
    const size_t SD = (size_t)S * D;
    u16* kv = ws; u16* Y = ws + SD; u16* x1 = ws + 2 * SD; u16* h = ws + 3 * SD; u16* o = ws + 4 * SD;
    u16* x2 = ws + 5 * SD; u16* x3 = ws + 6 * SD; u16* qkv = ws + 7 * SD;
    int nb = (S * D + 255) / 256;
    k_kv<<<nb, 256, 0, stream>>>(k, v, sk, kv);
    k_conv<<<nb, 256, 0, stream>>>(kv, hk, sd, Y);
    k_alayer<<<S, 64, 0, stream>>>(q, Y, x1);
    k_ln<<<S, 64, 0, stream>>>(x1, h);
    k_gemm<<<(S * 3 * D + 255) / 256, 256, 0, stream>>>(h, wqkv, nullptr, qkv, S, 3 * D, D);
    k_attn<<<S * H, 256, 0, stream>>>(qkv, o);
    k_gemm<<<nb, 256, 0, stream>>>(o, wo, x1, x2, S, D, D);
    k_alayer<<<S, 64, 0, stream>>>(x2, Y, x3);
    k_ln<<<S, 64, 0, stream>>>(x3, h);
    k_gemm<<<(S * 3 * D + 255) / 256, 256, 0, stream>>>(h, wqkv, nullptr, qkv, S, 3 * D, D);
    k_attn<<<S * H, 256, 0, stream>>>(qkv, o);
    k_gemm<<<nb, 256, 0, stream>>>(o, wo, x3, out, S, D, D);
}
```

```cpp
#include <hip/hip_runtime.h>
#include <stdint.h>
#include <type_traits>

namespace {
typedef unsigned short u16;
typedef unsigned u32;
typedef u32 v4u __attribute__((ext_vector_type(4)));
typedef u32 v2u __attribute__((ext_vector_type(2)));
typedef float f4 __attribute__((ext_vector_type(4)));
typedef float f2 __attribute__((ext_vector_type(2)));
typedef short s4 __attribute__((ext_vector_type(4)));
typedef __bf16 bf8 __attribute__((ext_vector_type(8)));
typedef __bf16 bf2 __attribute__((ext_vector_type(2)));
#define LAS __attribute__((address_space(3)))
#define GAS __attribute__((address_space(1)))
typedef LAS char lch;
typedef __amdgpu_buffer_rsrc_t rsrc_t;
#define DEV __device__ __forceinline__
#define RLX_AGENT __ATOMIC_RELAXED, __HIP_MEMORY_SCOPE_AGENT

constexpr u32 SD2 = 2048u * 448u * 2u;
constexpr u32 FP = 4352;
constexpr u32 XP = 16640, OP = 14592, KP = 8448, IP = 107776;
constexpr u32 WS_F1 = FP, WS_F2 = WS_F1 + 32 * FP, WS_KVT = 0xB0000, WS_YT = WS_KVT + SD2, WS_X2 = WS_YT + SD2,
              WS_O = WS_X2 + 128 * XP, WS_KV = WS_O + 256 * OP, WS_IMG = WS_KV + 1024 * KP, WS_END = WS_IMG + 16 * IP;
static_assert(WS_F2 + 128 * FP <= WS_KVT, "ws map");
constexpr u32 WS_TS = WS_END;
constexpr u32 L_W = 0, WSTEP = 5568, L_WO = 77 * 1024, L_HALO = L_WO + 28672, L_OWN = L_HALO + 32768,
              L_ST = L_OWN + 16384, L_CS = L_ST + 1024, L_CNT = L_CS + 384, L_SC7 = L_CNT + 64, L_END = L_SC7 + 2048;
static_assert(L_END <= 163840 && L_HALO + 256 == IP, "lds map");
constexpr u32 HSTR = 4096 + 64;
constexpr u32 BIG = 0x7ff00000u;
constexpr u32 SPIN = 1u << 20;

template <class T, class F> DEV T bc(F f) { return __builtin_bit_cast(T, f); }
DEV rsrc_t mkrs(const void* p, u32 bytes) { return __builtin_amdgcn_make_buffer_rsrc((void*)p, 0, bytes, 0x00020000); }
template <int AUX> DEV v4u ld16(rsrc_t r, u32 o) { return __builtin_amdgcn_raw_buffer_load_b128(r, o, 0, AUX); }
template <int AUX> DEV v2u ld8(rsrc_t r, u32 o) { return __builtin_amdgcn_raw_buffer_load_b64(r, o, 0, AUX); }
template <int AUX> DEV u32 ld4(rsrc_t r, u32 o) { return __builtin_amdgcn_raw_buffer_load_b32(r, o, 0, AUX); }
template <int AUX> DEV u32 ld2(rsrc_t r, u32 o) { return (u32)(u16)__builtin_amdgcn_raw_buffer_load_b16(r, o, 0, AUX); }
template <int AUX> DEV void st16(v4u v, rsrc_t r, u32 o) { __builtin_amdgcn_raw_buffer_store_b128(v, r, o, 0, AUX); }
template <int AUX> DEV void st8(v2u v, rsrc_t r, u32 o) { __builtin_amdgcn_raw_buffer_store_b64(v, r, o, 0, AUX); }
template <int AUX> DEV void st4(u32 v, rsrc_t r, u32 o) { __builtin_amdgcn_raw_buffer_store_b32(v, r, o, 0, AUX); }
template <int AUX> DEV v4u ld16s(rsrc_t r, u32 o, u32 so) { return __builtin_amdgcn_raw_buffer_load_b128(r, o, so, AUX); }
template <int AUX> DEV v2u ld8s(rsrc_t r, u32 o, u32 so) { return __builtin_amdgcn_raw_buffer_load_b64(r, o, so, AUX); }
template <int AUX> DEV u32 ld4s(rsrc_t r, u32 o, u32 so) { return __builtin_amdgcn_raw_buffer_load_b32(r, o, so, AUX); }
template <int AUX> DEV void st16s(v4u v, rsrc_t r, u32 o, u32 so) {
    __builtin_amdgcn_raw_buffer_store_b128(v, r, o, so, AUX);
    asm volatile("s_nop 1" ::"v"(v) : "memory");
}
template <int AUX> DEV void st8s(v2u v, rsrc_t r, u32 o, u32 so) { __builtin_amdgcn_raw_buffer_store_b64(v, r, o, so, AUX); }
template <int AUX> DEV void st4s(u32 v, rsrc_t r, u32 o, u32 so) { __builtin_amdgcn_raw_buffer_store_b32(v, r, o, so, AUX); }
template <int AUX> DEV v4u ldk(rsrc_t r, u32 lb, u32 so, int j) { return ld16s<AUX>(r, lb + (j & 3) * 1024, so + (j >> 2) * 4096); }
DEV void sb() { __builtin_amdgcn_sched_barrier(0); }
DEV f2 fm2(f2 a, f2 b, f2 c) { return __builtin_elementwise_fma(a, b, c); }
DEV f4 mma(v4u a, v4u b, f4 c) { return __builtin_amdgcn_mfma_f32_16x16x32_bf16(bc<bf8>(a), bc<bf8>(b), c, 0, 0, 0); }
DEV f4 mma16(v2u a, v2u b, f4 c) { return mma(v4u{a.x, a.y, 0, 0}, v4u{b.x, b.y, 0, 0}, c); }
DEV u32 pk(float lo, float hi) { f2 f = {lo, hi}; return bc<u32>(__builtin_convertvector(f, bf2)); }
DEV float blo(u32 d) { return bc<float>(d << 16); }
DEV float bhi(u32 d) { return bc<float>(d & 0xffff0000u); }
DEV float adj(float hi, u32 lo) { return bc<float>(bc<u32>(__builtin_fabsf(hi) < 1e-30f ? 1e-30f : hi) - lo); }
template <class T> DEV void pin(T& v) { asm volatile("" : "+v"(v)); }
DEV float fm(float a, float b, float c) { return __builtin_fmaf(a, b, c); }
DEV v4u lr16(lch* p) { return *(LAS v4u*)p; }
DEV v2u lr8(lch* p) { return *(LAS v2u*)p; }
DEV u32 lr4(lch* p) { return *(LAS u32*)p; }
DEV void lw16(lch* p, v4u v) { *(LAS v4u*)p = v; }
DEV void lw8(lch* p, v2u v) { *(LAS v2u*)p = v; }
DEV void lw4(lch* p, u32 v) { *(LAS u32*)p = v; }
DEV v2u ltr(lch* p) { return bc<v2u>(__builtin_amdgcn_ds_read_tr16_b64_v4i16((LAS s4*)p)); }
DEV void cbar() { asm volatile("" ::: "memory"); }
DEV void wgbar() { asm volatile("s_waitcnt lgkmcnt(0)\n\ts_barrier" ::: "memory"); }
DEV void drain() { asm volatile("s_waitcnt vmcnt(0)" ::: "memory"); }
DEV float rows_max(float v) {
    auto a = __builtin_amdgcn_permlane32_swap(bc<u32>(v), bc<u32>(v), false, false);
    v = fmaxf(bc<float>(a[0]), bc<float>(a[1]));
    auto b = __builtin_amdgcn_permlane16_swap(bc<u32>(v), bc<u32>(v), false, false);
    return fmaxf(bc<float>(b[0]), bc<float>(b[1]));
}
DEV float rows_sum(float v) {
    auto a = __builtin_amdgcn_permlane32_swap(bc<u32>(v), bc<u32>(v), false, false);
    v = bc<float>(a[0]) + bc<float>(a[1]);
    auto b = __builtin_amdgcn_permlane16_swap(bc<u32>(v), bc<u32>(v), false, false);
    return bc<float>(b[0]) + bc<float>(b[1]);
}
DEV float row3_bcast(float v) {
    auto a = __builtin_amdgcn_permlane32_swap(bc<u32>(v), bc<u32>(v), false, false);
    auto b = __builtin_amdgcn_permlane16_swap(a[1], a[1], false, false);
    return bc<float>(b[1]);
}
DEV void wait_flag(rsrc_t r, u32 off, u32 so, u32 tag) {
#pragma unroll 1
    for (u32 it = 0; it < SPIN; ++it) {
        u32 f = ld4s<16>(r, off, so);
        cbar();
        if (__all(f == tag)) break;
        __builtin_amdgcn_s_sleep(1);
    }
    cbar();
}
DEV void wait_flag4(rsrc_t r, u32 off, u32 so, u32 tag) {
#pragma unroll 1
    for (u32 it = 0; it < SPIN; ++it) {
        v4u f = ld16s<16>(r, off, so);
        cbar();
        if (__all(f.x == tag && f.y == tag && f.z == tag && f.w == tag)) break;
        __builtin_amdgcn_s_sleep(1);
    }
    cbar();
}
DEV void wait_lds(lch* p, u32 want) {
#pragma unroll 1
    for (u32 it = 0; it < SPIN * 8; ++it) {
        u32 f = *(volatile LAS u32*)p;
        if (__builtin_amdgcn_readfirstlane(f) >= want) break;
        __builtin_amdgcn_s_sleep(1);
    }
    cbar();
}
DEV void sig_lds(lch* p, int lane) {
    cbar();
    if (lane == 0) asm volatile("ds_add_u32 %0, %1" ::"v"((u32)(uintptr_t)p), "v"(1u) : "memory");
    cbar();
}
#define TSW(n) do { } while (0)
#define TC(n) do { } while (0)
#define TS(n) do { } while (0)

__global__ void __launch_bounds__(512, 2)
k_main(const u16* q, const u16* k, const u16* v, const u16* sk, const u16* hk, const u16* sd,
       const u16* wqkv, const u16* wo, u16* out, char* ws) {
    extern __shared__ __attribute__((aligned(16))) char smem_[];
    lch* const L = (lch*)smem_;
    const int tid = threadIdx.x, lane = tid & 63;
    const int wv = __builtin_amdgcn_readfirstlane(tid >> 6);
    const int w = blockIdx.x, g = w & 7, i = w >> 3, hd = i & 15;
    const int X = wv >> 2, wq = wv & 3, tau = 2 * g + (i >> 4) + 16 * X, b = 4 * tau + wq;
    const int n0 = lane & 15, gq = lane >> 4;
    const u32 lb = lane * 16;
    const u32 l0 = lane == 0 ? 0u : BIG;
    const bool conv = i < 28;
    const int jr = hd < 14 ? hd : 13;
    const rsrc_t rws = mkrs(ws, WS_END);
    const v4u ones = {0x3f803f80u, 0x3f803f80u, 0x3f803f80u, 0x3f803f80u};
    lch* const cnt = L + L_CNT;
    lch* const own = L + L_OWN + X * 8192;
    lch* const sc0 = L + (wv < 7 ? L_HALO + 4 * HSTR + wv * 2048 : L_SC7);
    TS(0);
    TC(30);

    u32 kk[11], vv[8];
    v4u skw, w0, w1;
    v4u xf[15], yf[15];
    const rsrc_t rq = mkrs(q, SD2);
    auto load_q = [&]() {
        const u32 o = (u32)((lane >> 3) * 896 + (lane & 7) * 16);
#pragma unroll
        for (int s = 0; s < 14; ++s) yf[s] = ld16s<0>(rq, o + (s >> 1) * 128, (u32)b * 14336 + (s & 1) * 7168);
    };
    const int task = w * 4 + wq, th = task / 56, ti = task - th * 56, tm = ti / 14, tsn = ti - tm * 14;
    const bool iso = tm == 3;
    if (X == 0) {
        const rsrc_t rk = mkrs(k, SD2), rv = mkrs(v, SD2), rsk = mkrs(sk, 3584);
        const int tg = lane >> 3, cp = 8 * wv + (lane & 7), t0 = 64 * i + 8 * tg, ch = 56 * g + 2 * cp;
        const bool act = cp < 28;
        const u32 ob = act ? (u32)(t0 * 896 + ch * 2) : BIG;
        const u32 oa = (act && t0) ? ob - 2688u : BIG;
#pragma unroll
        for (int j = 0; j < 3; ++j) kk[j] = ld4<0>(rk, oa + j * 896);
#pragma unroll
        for (int j = 0; j < 8; ++j) kk[3 + j] = ld4<0>(rk, ob + j * 896);
#pragma unroll
        for (int j = 0; j < 8; ++j) vv[j] = ld4<0>(rv, ob + j * 896);
        skw = ld16<0>(rsk, act ? (u32)ch * 8 : BIG);
    } else {
        const rsrc_t rw = mkrs(iso ? wo : wqkv, th >= 16 ? 0u : iso ? 448u * 896u : 448u * 2688u);
        const int r = lane >> 2, e = r & 7;
        u32 o[2];
#pragma unroll
        for (int u = 0; u < 2; ++u) {
            const int c = 4 * tsn + 2 * u + (r >> 3), h = (c * 43) >> 7;
            const int f = c < 48 ? h * 16 + 4 * c + (e & 3) + (e >> 2) * 16 : (2 * (c - 48) + (e >> 2)) * 28 + 12 + (e & 3);
            o[u] = (iso ? f * 896 + (th < 14 ? th : 13) * 64 : (32 * tsn + 16 * u + r) * 2688 + tm * 896 + th * 56) + (lane & 3) * 16;
        }
        w0 = ld16<0>(rw, o[0]);
        w1 = ld16<0>(rw, o[1]);
    }
    const u32 epoch = ld4<16>(rws, 0);
    const int cch = X, pp = wq, cc = 56 * g + 2 * i + cch;
    v4u h0;
    u32 h1, dd;
    {
        const rsrc_t rh = mkrs(hk, SD2);
        const u32 ho = conv ? (u32)(cc * 4096 + pp * 1024) + lb : BIG;
        h0 = ld16<0>(rh, ho);
        h1 = ld4<0>(rh, (tid & 255) ? ho - 4 : BIG);
        dd = ld4<0>(mkrs(sd, 896), (u32)(cc - cch) * 2);
    }
    if (tid < 64) lw16(L + L_OWN + (tid >> 5) * 4608 + 4096 + (tid & 31) * 16, v4u{0, 0, 0, 0});
    if (tid >= 64 && tid < 80) lw4(L + L_CNT + (tid - 64) * 4, 0);
    if (tid >= 128 && tid < 136) lw16(L + L_HALO + ((tid - 128) >> 1) * HSTR + (tid & 1) * 16, v4u{0, 0, 0, 0});
    u32 tag;
    if (X == 0) {
        tag = __builtin_amdgcn_readfirstlane(epoch) + 1u;
        asm volatile("" ::"s"(tag));
        wgbar();
        TS(24);
        const int c1 = lane & 7, t1 = lane >> 3;
        const int c2 = lane >> 3, t2 = 7 - (lane & 7);
        v4u o[2];
#pragma unroll
        for (int h = 0; h < 2; ++h) {
            float kf[11], r[8];
#pragma unroll
            for (int j = 0; j < 11; ++j) kf[j] = h ? bhi(kk[j]) : blo(kk[j]);
            const float w0 = blo(skw[2 * h]), w1 = bhi(skw[2 * h]), w2 = blo(skw[2 * h + 1]), w3 = bhi(skw[2 * h + 1]);
#pragma unroll
            for (int j = 0; j < 8; ++j)
                r[j] = fm(kf[j + 3], w0, fm(kf[j + 2], w1, fm(kf[j + 1], w2, kf[j] * w3))) * (h ? bhi(vv[j]) : blo(vv[j]));
            lw16(sc0 + (c1 * 8 + (t1 ^ c1)) * 16, v4u{pk(r[7], r[6]), pk(r[5], r[4]), pk(r[3], r[2]), pk(r[1], r[0])});
            o[h] = lr16(sc0 + (c2 * 8 + (t2 ^ c2)) * 16);
        }
        const u32 po = 8 * wv + c2 < 28 ? (u32)(c2 * 8192 + (lane & 7) * 16) : BIG;
        const u32 so = WS_KVT + (u32)((56 * g + 16 * wv) * 4096 + (1984 - 64 * i) * 2);
        st16s<16>(o[0], rws, po, so);
        st16s<16>(o[1], rws, po, so + 4096);
        TS(27);
        drain();
        st4s<16>(tag, rws, l0, WS_F1 + (u32)(g * 4 + wv) * FP + i * 4);
        TS(1);
        load_q();
    } else {
        __builtin_amdgcn_s_sleep(8);
        load_q();
        tag = __builtin_amdgcn_readfirstlane(epoch) + 1u;
        asm volatile("" ::"s"(tag));
        wgbar();
        TS(24);
        const u32 tro = (u32)((8 * gq + (n0 >> 2)) * 64 + (iso ? (n0 & 3) * 16 : (n0 & 3) * 8));
        const u32 ctb = iso ? 8 : 32;
        lw16(sc0 + lb, w0);
        lw16(sc0 + 1024 + lb, w1);
        const v2u a0 = ltr(sc0 + tro), a1 = ltr(sc0 + tro + 256), b0 = ltr(sc0 + tro + ctb), b1 = ltr(sc0 + tro + ctb + 256);
        if (th < 16) {
            const u32 so = WS_IMG + (u32)th * IP + (iso ? L_WO + tsn * 2048 : tsn * WSTEP);
            st16s<16>(v4u{a0.x, a0.y, a1.x, a1.y}, rws, lb, so + (iso ? 0 : tm * 1024));
            st16s<16>(!iso && n0 >= 12 ? ones : v4u{b0.x, b0.y, b1.x, b1.y}, rws,
                      iso ? lb : n0 <= 12 ? (u32)(gq * 13 + n0) * 16 : BIG, so + (iso ? 1024 : 3072 + tm * 832));
            TS(27);
            drain();
            st4s<16>(tag, rws, l0, WS_IMG + (u32)th * IP + L_HALO + ti * 4);
        }
        TS(1);
    }

    v4u ha[2][8], ub_[2][8], uv;
    lch* const hp = L + L_HALO + cch * 2 * HSTR;
    if (conv) {
        lw16(hp + 32 + pp * 1024 + lb, h0);
        lw16(hp + HSTR + 32 + pp * 1024 + lb,
             v4u{__builtin_amdgcn_alignbit(h0.x, h1, 16), __builtin_amdgcn_alignbit(h0.y, h0.x, 16),
                 __builtin_amdgcn_alignbit(h0.z, h0.y, 16), __builtin_amdgcn_alignbit(h0.w, h0.z, 16)});
        wgbar();
        lch* p = hp + ((n0 & 1) ? 0 : HSTR) + (((n0 + 2) & ~1) + 64 * pp + 8 * gq) * 2;
#pragma unroll
        for (int c = 0; c < 8; ++c)
#pragma unroll
            for (int e = 0; e < 2; ++e)
#pragma unroll
                for (int m = 0; m < 4; ++m) ha[e][c][m] = lr4(p + c * 512 + e * 64 + m * 4);
    }
    float xr, xc;
    {
        lch* pw = sc0 + ((lane & 7) * 16 + ((lane >> 3) ^ (lane & 7))) * 16;
        lch* pr0 = sc0 + (gq * 16 + (n0 ^ gq)) * 16;
        lch* pr1 = sc0 + ((gq + 4) * 16 + (n0 ^ (gq + 4))) * 16;
#pragma unroll
        for (int j = 0; j < 7; ++j) {
            lw16(pw, yf[2 * j]);
            lw16(pw + 128, yf[2 * j + 1]);
            xf[2 * j] = lr16(pr0);
            xf[2 * j + 1] = lr16(pr1);
            if (j == 0 || j == (jr >> 1)) xf[14] = lr16((jr & 1) ? pr1 : pr0);
        }
        float s1 = 0.f, s2 = 0.f;
#pragma unroll
        for (int s = 0; s < 14; ++s)
#pragma unroll
            for (int m = 0; m < 4; ++m) {
                s1 = __builtin_amdgcn_fdot2_f32_bf16(bc<bf2>(xf[s][m]), bc<bf2>(0x3f803f80u), s1, false);
                s2 = __builtin_amdgcn_fdot2_f32_bf16(bc<bf2>(xf[s][m]), bc<bf2>(xf[s][m]), s2, false);
            }
        s1 = rows_sum(s1) * (1.f / 448);
        s2 = rows_sum(s2) * (1.f / 448);
        xr = __builtin_amdgcn_rsqf(fmaxf(s2 - s1 * s1, 0.f) + 1e-5f);
        xc = -s1 * xr;
        pin(xr);
        pin(xc);
        TS(25);
    }
    if (conv) {
        wait_flag(rws, (lane & 7) * 4, WS_F1 + (u32)(g * 4 + (i >> 3)) * FP + (24 - 8 * pp) * 4, tag);
        TS(2);
        uv = ld16s<16>(rws, lb, WS_KVT + (u32)(cc * 4096 + pp * 1024));
    }
    const u32 img = WS_IMG + (u32)hd * IP;
    wait_flag(rws, (lane < 56 ? lane : 0) * 4, img + L_HALO, tag);
    TS(26);
    v4u wb[10];
#pragma unroll
    for (int j = 0; j < 5; ++j) wb[j] = ldk<16>(rws, lb, img + wv * 5120, j);
    lch* const U = L + L_OWN + cch * 4608;
    lch* const ub = U + (2032 - 1792 + 64 * pp - 16 * n0 + 8 * gq) * 2;
    lch* const rw = L + L_HALO + wv * 1024 + (gq * 16 + (n0 ^ (gq * 4))) * 16;
    auto finish = [&]() {
        const int col = lane >> 2, qd = lane & 3;
        lch* rr = L + L_HALO + wq * 8192 + (qd * 16 + (col ^ (qd * 4))) * 16;
        f4 p[8];
        v2u u[2];
#pragma unroll
        for (int j = 0; j < 8; ++j) p[j] = bc<f4>(lr16(rr + j * 1024));
#pragma unroll
        for (int h = 0; h < 2; ++h) u[h] = lr8(L + L_OWN + h * 4608 + (2044 - 256 * wv - 4 * lane) * 2);
        sb();
        f4 y[2];
#pragma unroll
        for (int h = 0; h < 2; ++h) {
            const float d = h ? bhi(dd) : blo(dd);
            y[h] = (p[4 * h] + p[4 * h + 1]) + (p[4 * h + 2] + p[4 * h + 3]);
            y[h][0] = fm(bhi(u[h].y), d, y[h][0]);
            y[h][1] = fm(blo(u[h].y), d, y[h][1]);
            y[h][2] = fm(bhi(u[h].x), d, y[h][2]);
            y[h][3] = fm(blo(u[h].x), d, y[h][3]);
        }
        const u32 a = pk(y[0][0], y[0][1]), c = pk(y[0][2], y[0][3]);
        st16s<16>(v4u{a, c, pk(adj(y[1][0], a & 0xffff), adj(y[1][1], a >> 16)), pk(adj(y[1][2], c & 0xffff), adj(y[1][3], c >> 16))},
                  rws, (u32)(col * 14336 + qd * 16), WS_YT + (u32)(16 * wv * 14336 + (28 * g + i) * 64));
    };
    auto flag2 = [&]() {
        drain();
        st4s<16>(tag, rws, lane < 2 ? (u32)lane * 8 * FP : BIG, WS_F2 + (u32)(wv * 16 + g) * FP + i * 4);
        TS(4);
    };
    f4 acc[4];
    auto macs = [&](int part, int s0, int s1) {
#pragma unroll
        for (int s = s0; s < s1; ++s)
#pragma unroll
            for (int e = 0; e < 2; ++e)
#pragma unroll
                for (int t = 0; t < 4; ++t) {
                    const int c = 4 * part + t - s;
                    if (c >= 0) acc[t] = mma(ha[e][c], ub_[e][s], s + e ? acc[t] : f4{0, 0, 0, 0});
                }
    };
    if (conv) {
        lw16(U + pp * 1024 + lb, uv);
        wgbar();
        TS(3);
#pragma unroll
        for (int s = 0; s < 4; ++s)
#pragma unroll
            for (int e = 0; e < 2; ++e) ub_[e][s] = lr16(ub + (7 - s) * 512 + e * 64);
        sb();
        macs(0, 0, 4);
#pragma unroll
        for (int t = 0; t < 4; ++t) lw16(rw + t * 8192, bc<v4u>(acc[t]));
        wgbar();
        if (X == 0) finish();
        sb();
        macs(1, 0, 3);
#pragma unroll
        for (int t = 0; t < 4; ++t) pin(acc[t]);
#pragma unroll
        for (int s = 4; s < 8; ++s)
#pragma unroll
            for (int e = 0; e < 2; ++e) ub_[e][s] = lr16(ub + (7 - s) * 512 + e * 64);
    }
#pragma unroll
    for (int j = 0; j < 5; ++j) lw16(L + wv * 5120 + j * 1024 + lb, wb[j]);
    if (conv && X == 0) flag2();
    const int pb = X ? 40 + 10 * wq : 77 + 7 * wq;
    const bool ten = X && wq < 3;
#pragma unroll
    for (int j = 0; j < 7; ++j) wb[j] = ldk<16>(rws, lb, img + pb * 1024, j);
    if (ten) {
#pragma unroll
        for (int j = 7; j < 10; ++j) wb[j] = ldk<16>(rws, lb, img + pb * 1024, j);
    }
    if (conv) {
        sb();
        macs(1, 3, 8);
#pragma unroll
        for (int t = 0; t < 4; ++t) pin(acc[t]);
        wgbar();
#pragma unroll
        for (int t = 0; t < 4; ++t) lw16(rw + t * 8192, bc<v4u>(acc[t]));
        wgbar();
        if (X == 1) finish();
    }
    wgbar();
#pragma unroll
    for (int j = 0; j < 7; ++j) lw16(L + pb * 1024 + j * 1024 + lb, wb[j]);
    if (ten) {
#pragma unroll
        for (int j = 7; j < 10; ++j) lw16(L + pb * 1024 + j * 1024 + lb, wb[j]);
    }
    sig_lds(cnt + 24, lane);
    if (conv && X == 1) flag2();
    TS(5);
    lch* const wl = L + L_W + lb;
    lch* const wh = L + L_W + 3072 + (gq * 13 + (n0 < 12 ? n0 : 12)) * 16;
    wait_lds(cnt + 24, 8);
    if (ten) {
        f4 c0 = {0, 0, 0, 0}, c1 = c0;
#pragma unroll
        for (int s = 0; s < 14; ++s) {
            c0 = mma(lr16(wl + wq * 1024 + s * WSTEP), ones, c0);
            c1 = mma(lr16(wh + wq * 832 + s * WSTEP), ones, c1);
        }
        if (n0 == 0) {
            lw16(L + L_CS + wq * 128 + gq * 16, bc<v4u>(c0));
            lw16(L + L_CS + wq * 128 + 64 + gq * 16, bc<v4u>(c1));
        }
        sig_lds(cnt + 28, lane);
    }

    f4 aq0, aq1, ak0, ak1, av0, av1, ag;
    v4u res;
    v4u wf[2][6];
    auto ldw = [&](int s, int j) {
        wf[s & 1][j] = lr16(((j & 1) ? wh + (j >> 1) * 832 : wl + (j >> 1) * 1024) + s * WSTEP);
    };
    auto qkv = [&](auto first_, const v4u* t) {
        constexpr bool FIRST = decltype(first_)::value;
        lch* sc = own + (wq >> 1) * 4096 + (wq & 1) * 1024;
        lch* tr = sc + gq * 256 + (n0 >> 3) * 64 + (n0 & 3) * 16 + ((n0 >> 2) & 1) * 8;
        auto ytr = [&](int s) {
            lw16(sc + lb, t[s]);
            const v2u a = ltr(tr), c = ltr(tr + 128);
            yf[s] = v4u{a.x, a.y, c.x, c.y};
        };
        float xl[4], xh[4], yl[4];
        auto unp = [&](int s, int j) {
            if (j < 4) {
                xl[j] = blo(xf[s][j]);
                yl[j] = blo(yf[s][j]);
                pin(xl[j]);
                pin(yl[j]);
            } else if (FIRST) {
                xh[2 * j - 8] = bhi(xf[s][2 * j - 8]);
                xh[2 * j - 7] = bhi(xf[s][2 * j - 7]);
                pin(xh[2 * j - 8]);
                pin(xh[2 * j - 7]);
            }
        };
        auto gate = [&](int s) {
            const f2 r2 = {xr, xr}, c2 = {xc, xc};
            f2 x[4], y[4], z[4];
#pragma unroll
            for (int h = 0; h < 2; ++h) {
                x[h] = f2{xl[2 * h], xl[2 * h + 1]};
                y[h] = f2{yl[2 * h], yl[2 * h + 1]};
                x[2 + h] = FIRST ? f2{xh[2 * h], xh[2 * h + 1]} : f2{bc<float>(xf[s][2 * h]), bc<float>(xf[s][2 * h + 1])};
                y[2 + h] = f2{bc<float>(yf[s][2 * h]), bc<float>(yf[s][2 * h + 1])};
            }
#pragma unroll
            for (int h = 0; h < 4; ++h) z[h] = fm2(x[h], r2, c2);
            sb();
#pragma unroll
            for (int h = 0; h < 4; ++h) z[h] = fm2(z[h], y[h], x[h]);
            sb();
            return v4u{pk(z[0].x, z[2].x), pk(z[0].y, z[2].y), pk(z[1].x, z[3].x), pk(z[1].y, z[3].y)};
        };
        if (FIRST) {
            ytr(0);
            ytr(1);
        }
#pragma unroll
        for (int j = 0; j < 6; ++j) unp(0, j);
        sb();
        const f4 z4 = {0, 0, 0, 0};
#pragma unroll
        for (int s = 0; s < 14; ++s) {
            v4u xp = gate(s);
            pin(xp);
            sb();
            auto slot = [&](int j) {
                if (s < 13) ldw(s + 1, j);
                unp(s + 1, j);
                sb();
            };
            ak0 = mma(wf[s & 1][2], xp, s ? ak0 : z4);
            slot(0);
            ak1 = mma(wf[s & 1][3], xp, s ? ak1 : z4);
            slot(1);
            av0 = mma(xp, wf[s & 1][4], s ? av0 : z4);
            slot(2);
            av1 = mma(xp, wf[s & 1][5], s ? av1 : z4);
            slot(3);
            ag = mma(xp, xp, s ? ag : z4);
            slot(4);
            aq0 = mma(wf[s & 1][0], xp, s ? aq0 : z4);
            slot(5);
            aq1 = mma(wf[s & 1][1], xp, s ? aq1 : z4);
            if (FIRST && s < 13) ytr(s + 2);
            sb();
        }
        res = gate(14);
        pin(res);
    };
    {
#pragma unroll
        for (int j = 0; j < 6; ++j) ldw(0, j);
        if (wq == 3) {
            wait_flag4(rws, (u32)(lane >> 3) * FP + ((lane & 7) < 7 ? lane & 7 : 0) * 16,
                       WS_F2 + (u32)(((tau >> 2) * 2 + (g & 1)) * 8) * FP, tag);
            sig_lds(cnt + X * 4, lane);
            if (w == 0 && X == 0) st4<16>(tag, rws, l0);
        } else {
            wait_lds(cnt + X * 4, 1);
        }
        TS(6);
        v4u t[15];
        const u32 so = WS_YT + (u32)b * 14336;
#pragma unroll
        for (int s = 0; s < 14; ++s) {
            t[s] = ldk<16>(rws, lb, so, s);
            sb();
        }
        t[14] = ld16s<16>(rws, lb, so + jr * 1024);
        sb();
        qkv(std::true_type{}, t);
        wait_lds(cnt + 28, 3);
    }

#pragma unroll 1
    for (int ly = 0;; ++ly) {
        TS(8 + 8 * ly);
        v4u qf;
        const u32 kvo = WS_KV + (u32)((ly * 16 + hd) * 32 + tau) * KP;
        {
            const f4 cq0 = bc<f4>(lr16(L + L_CS + gq * 16)), cq1 = bc<f4>(lr16(L + L_CS + 64 + gq * 16));
            const f4 ck0 = bc<f4>(lr16(L + L_CS + 128 + gq * 16)), ck1 = bc<f4>(lr16(L + L_CS + 192 + gq * 16));
            const float cv0 = bc<float>(lr4(L + L_CS + 256 + n0 * 4)), cv1 = bc<float>(lr4(L + L_CS + 320 + n0 * 4));
            lch* st = L + L_ST + wv * 128;
            const int e = n0 & 3;
            const float dg = (e & 2) ? ((e & 1) ? ag[3] : ag[2]) : ((e & 1) ? ag[1] : ag[0]);
            if (gq == 3) lw4(st + n0 * 8, bc<u32>(ak1[0]));
            if (gq == (n0 >> 2)) lw4(st + n0 * 8 + 4, bc<u32>(dg));
            cbar();
            const f2 so = bc<f2>(lr8(st + n0 * 8));
            const f4 sa = bc<f4>(lr16(st + gq * 32)), sb_ = bc<f4>(lr16(st + gq * 32 + 16));
            auto stat = [](float a, float c, float& nm, float& r) {
                nm = a * (-1.f / 448);
                r = __builtin_amdgcn_rsqf(fmaxf(fm(-nm, nm, c * (1.f / 448)), 0.f) + 1e-5f);
            };
            float nm, r, vm[4], vr[4];
            stat(so.x, so.y, nm, r);
            float t0[4], t1[4];
#pragma unroll
            for (int e = 0; e < 4; ++e) { t0[e] = fm(nm, ck0[e], ak0[e]) * r; t1[e] = fm(nm, ck1[e], ak1[e]) * r; }
            v4u kf = {pk(t0[0], t0[1]), pk(t0[2], t0[3]), gq == 3 ? 0u : pk(t1[0], t1[1]), gq == 3 ? 0u : pk(t1[2], t1[3])};
            st16s<16>(kf, rws, lb, kvo + wq * 2048);
            stat(sa[0], sa[1], vm[0], vr[0]);
            stat(sa[2], sa[3], vm[1], vr[1]);
            stat(sb_[0], sb_[1], vm[2], vr[2]);
            stat(sb_[2], sb_[3], vm[3], vr[3]);
#pragma unroll
            for (int e = 0; e < 4; ++e) { t0[e] = fm(vm[e], cv0, av0[e]) * vr[e]; t1[e] = fm(vm[e], cv1, av1[e]) * vr[e]; }
            v4u vf = {pk(t0[0], t0[1]), pk(t0[2], t0[3]), n0 >= 12 ? 0x3f803f80u : pk(t1[0], t1[1]),
                      n0 >= 12 ? 0x3f803f80u : pk(t1[2], t1[3])};
            st16s<16>(vf, rws, lb + 1024, kvo + wq * 2048);
            sb();
            if (ly) wait_lds(cnt + 16 + X * 4, 4);
            lch* ob = own + (wq >> 1) * 4096 + lb;
            lw16(ob + (wq & 1) * 1024, kf);
            lw8(ob + 2048 + (wq & 1) * 8, v2u{vf.x, vf.y});
            lw8(ob + 3072 + (wq & 1) * 8, v2u{vf.z, vf.w});
            const float rq = r * (0.18898223650461362f * 1.4426950408889634f);
#pragma unroll
            for (int e = 0; e < 4; ++e) { t0[e] = fm(nm, cq0[e], aq0[e]) * rq; t1[e] = fm(nm, cq1[e], aq1[e]) * rq; }
            qf = v4u{pk(t0[0], t0[1]), pk(t0[2], t0[3]), gq == 3 ? 0x3f80u : pk(t1[0], t1[1]), gq == 3 ? 0u : pk(t1[2], t1[3])};
        }
        pin(qf);
        drain();
        st4s<16>(tag, rws, l0, kvo + 8192 + wq * 4);
        TS(9 + 8 * ly);
        {
            const int tp = tau - 4 + wq;
            v4u hb[8];
            if (tp >= 0) {
                const u32 so = kvo - (u32)(4 - wq) * KP;
                wait_flag(rws, (lane & 3) * 4, so + 8192, tag);
                TS(10 + 8 * ly);
#pragma unroll
                for (int j = 0; j < 8; ++j) hb[j] = ldk<16>(rws, lb, so, j);
            } else {
#pragma unroll
                for (int j = 0; j < 8; ++j) hb[j] = v4u{0, 0, (j & 1) == 0 && gq == 3 ? 0xf149u : 0u, 0};
            }
            if (X + ly) wait_lds(cnt + 16 + (X ^ 1) * 4, 4 * (ly + X));
            lch* hp = L + L_HALO + wq * 8192 + lb;
#pragma unroll
            for (int j = 0; j < 4; ++j) {
                lch* p = hp + (j >> 1) * 4096;
                lw16(p + (j & 1) * 1024, hb[2 * j]);
                lw8(p + 2048 + (j & 1) * 8, v2u{hb[2 * j + 1].x, hb[2 * j + 1].y});
                lw8(p + 3072 + (j & 1) * 8, v2u{hb[2 * j + 1].z, hb[2 * j + 1].w});
            }
            sig_lds(cnt + 8 + X * 4, lane);
        }
        const u32 oo = WS_O + (u32)(ly * 128 + b) * OP;
        {
            const int P0 = (wq + 1) >> 1, par = wq & 1;
            auto blk = [&](int P) { return P < 8 ? L + L_HALO + P * 4096 + lb : own + (P - 8) * 4096 + lb; };
            lch* a0 = blk(P0);
            lch* a6 = blk(P0 + 6);
            lch* a7 = blk(P0 + 7);
            lch* ao = blk(par ? wq >> 1 : 8 + (wq >> 1));
            int dl = n0 - 4 * gq;
            asm volatile("" : "+v"(dl));
            f4 mf, ml, mo;
#pragma unroll
            for (int e = 0; e < 4; ++e) {
                const bool gt = e > dl;
                mf[e] = (par || gt) ? 0.f : -1e30f;
                ml[e] = (!par || !gt) ? 0.f : -1e30f;
                mo[e] = (gt == (bool)par) ? 0.f : -1e30f;
            }
            asm volatile("" : "+v"(mf), "+v"(ml), "+v"(mo), "+v"(a0), "+v"(a6), "+v"(a7), "+v"(ao));
            wait_lds(cnt + 8 + X * 4, 4 * (ly + 1));
            TS(11 + 8 * ly);
            v4u kf[17];
            kf[16] = lr16(ao + par * 1024);
#pragma unroll
            for (int u = 0; u < 16; ++u) kf[u] = lr16((u < 12 ? a0 + (u >> 1) * 4096 : u < 14 ? a6 : a7) + (u & 1) * 1024);
            sb();
            f4 s[17];
            s[16] = mma(kf[16], qf, mo);
#pragma unroll
            for (int u = 0; u < 16; ++u) s[u] = mma(kf[u], qf, u == 0 ? mf : u == 15 ? ml : f4{0, 0, 0, 0});
            v2u vo0 = lr8(ao + 2048 + par * 8), vo1 = lr8(ao + 3072 + par * 8);
            v4u vf[8][2];
#pragma unroll
            for (int u = 0; u < 8; ++u) {
                lch* p = u < 6 ? a0 + u * 4096 : u == 6 ? a6 : a7;
                vf[u][0] = lr16(p + 2048);
                vf[u][1] = lr16(p + 3072);
            }
            float mx = fmaxf(fmaxf(s[16][0], s[16][1]), fmaxf(s[16][2], s[16][3]));
#pragma unroll
            for (int u = 0; u < 16; ++u) mx = fmaxf(fmaxf(fmaxf(fmaxf(mx, s[u][0]), s[u][1]), s[u][2]), s[u][3]);
#pragma unroll
            for (int j = 0; j < 17; ++j) {
                __builtin_amdgcn_sched_group_barrier(0x8, 1, 0);
                __builtin_amdgcn_sched_group_barrier(0x100, 1, 0);
                if (j >= 2) __builtin_amdgcn_sched_group_barrier(0x2, 2, 0);
            }
            sb();
            sig_lds(cnt + 16 + X * 4, lane);
            mx = rows_max(mx);
            auto pe = [&](int u) {
                return v2u{pk(__builtin_amdgcn_exp2f(s[u][0] - mx), __builtin_amdgcn_exp2f(s[u][1] - mx)),
                           pk(__builtin_amdgcn_exp2f(s[u][2] - mx), __builtin_amdgcn_exp2f(s[u][3] - mx))};
            };
            f4 o0 = {0, 0, 0, 0}, o1 = o0;
            {
                const v2u p = pe(16);
                o0 = mma16(vo0, p, o0);
                o1 = mma16(vo1, p, o1);
            }
#pragma unroll
            for (int u = 0; u < 8; ++u) {
                const v2u pa = pe(2 * u), pb = pe(2 * u + 1);
                const v4u pf = {pa.x, pa.y, pb.x, pb.y};
                o0 = mma(vf[u][0], pf, o0);
                o1 = mma(vf[u][1], pf, o1);
            }
            const float inv = __builtin_amdgcn_rcpf(row3_bcast(o1[0]));
            o0 *= inv;
            o1 *= inv;
            const v4u of = {pk(o0[0], o0[1]), pk(o0[2], o0[3]), pk(o1[0], o1[1]), pk(o1[2], o1[3])};
            st16s<16>(of, rws, gq < 3 ? lb : BIG, oo + hd * 768);
            st8s<16>(v2u{of.x, of.y}, rws, gq == 3 ? (u32)n0 * 8 : BIG, oo + 12288 + hd * 128);
            TS(12 + 8 * ly);
        }
        const u32 xo_ = WS_X2 + (u32)b * XP;
        {
            lch* wp = L + L_WO + lb;
            v4u wg[28];
#pragma unroll
            for (int j = 0; j < 16; ++j) wg[j] = lr16(wp + j * 1024);
            drain();
            st4s<16>(tag, rws, l0, oo + 14336 + hd * 4);
            TS(13 + 8 * ly);
            TC(31);
            if (hd < 14) {
                wait_flag(rws, n0 * 4, oo + 14336, tag);
                TS(14 + 8 * ly);
                v4u of[14];
#pragma unroll
                for (int s = 0; s < 12; ++s) of[s] = ldk<16>(rws, lb, oo, s);
#pragma unroll
                for (int s = 12; s < 14; ++s) {
                    const v2u a = ld8s<16>(rws, gq * 256 + n0 * 8 + (s - 12) * 1024, oo + 12288);
                    const v2u c = ld8s<16>(rws, gq * 256 + n0 * 8 + (s - 12) * 1024 + 128, oo + 12288);
                    of[s] = v4u{a.x, a.y, c.x, c.y};
                }
                sb();
                f4 c0 = {blo(res.x), bhi(res.x), blo(res.y), bhi(res.y)}, c1 = {blo(res.z), bhi(res.z), blo(res.w), bhi(res.w)};
#pragma unroll
                for (int s = 0; s < 14; ++s) {
                    c0 = mma(wg[2 * s], of[s], c0);
                    if (2 * s + 16 < 28) wg[2 * s + 16] = lr16(wp + (2 * s + 16) * 1024);
                    c1 = mma(wg[2 * s + 1], of[s], c1);
                    if (2 * s + 17 < 28) wg[2 * s + 17] = lr16(wp + (2 * s + 17) * 1024);
                    sb();
                }
                if (ly == 0) {
                    const u32 e0 = pk(c0[0], c0[2]), e1 = pk(c1[0], c1[2]);
                    st16s<16>(v4u{pk(c0[0], adj(c0[1], e0 & 0xffff)), pk(c0[2], adj(c0[3], e0 >> 16)),
                                  pk(c1[0], adj(c1[1], e1 & 0xffff)), pk(c1[2], adj(c1[3], e1 >> 16))},
                              rws, lb, xo_ + hd * 1024);
                    const f4 a1 = c0 + c1, a2 = c0 * c0 + c1 * c1;
                    const float s1 = rows_sum((a1[0] + a1[1]) + (a1[2] + a1[3]));
                    const float s2 = rows_sum((a2[0] + a2[1]) + (a2[2] + a2[3]));
                    st8s<16>(v2u{bc<u32>(s1), bc<u32>(s2)}, rws, gq == 0 ? (u32)n0 * 8 : BIG, xo_ + 14336 + hd * 128);
                    drain();
                    st4s<16>(tag, rws, l0, xo_ + 16128 + hd * 4);
                } else {
                    st16s<0>(v4u{pk(c0[0], c0[1]), pk(c0[2], c0[3]), pk(c1[0], c1[1]), pk(c1[2], c1[3])}, mkrs(out, SD2),
                             (u32)(n0 * 896 + gq * 16), (u32)(b * 14336 + hd * 64));
                }
                TS(15 + 8 * ly);
            }
        }
        if (ly) break;
        {
#pragma unroll
            for (int j = 0; j < 6; ++j) ldw(0, j);
            wait_flag(rws, (lane < 14 ? lane : 0) * 4, xo_ + 16128, tag);
            TS(7);
            v2u sp[4];
#pragma unroll
            for (int m = 0; m < 4; ++m)
                sp[m] = ld8s<16>(rws, 4 * m + gq < 14 ? (u32)(lane * 8 + m * 512) : BIG, xo_ + 14336);
            sb();
#pragma unroll
            for (int s = 0; s < 14; ++s) {
                xf[s] = ldk<16>(rws, lb, xo_, s);
                sb();
            }
            xf[14] = ld16s<16>(rws, lb, xo_ + jr * 1024);
            sb();
            float s1 = (bc<float>(sp[0].x) + bc<float>(sp[1].x)) + (bc<float>(sp[2].x) + bc<float>(sp[3].x));
            float s2 = (bc<float>(sp[0].y) + bc<float>(sp[1].y)) + (bc<float>(sp[2].y) + bc<float>(sp[3].y));
            s1 = rows_sum(s1) * (1.f / 448);
            s2 = rows_sum(s2) * (1.f / 448);
            xr = __builtin_amdgcn_rsqf(fmaxf(s2 - s1 * s1, 0.f) + 1e-5f);
            xc = -s1 * xr;
#pragma unroll
            for (int s = 0; s < 15; ++s) pin(yf[s]);
            qkv(std::false_type{}, nullptr);
        }
    }
}
}

extern "C" void kernel_launch(void* const* d_in, const int* in_sizes, int n_in, void* d_out, int out_size,
                              void* d_ws, size_t ws_size, hipStream_t stream) {
    (void)in_sizes; (void)n_in; (void)out_size; (void)ws_size;
    (void)hipFuncSetAttribute((const void*)k_main, hipFuncAttributeMaxDynamicSharedMemorySize, (int)L_END);
    hipLaunchKernelGGL(k_main, dim3(256), dim3(512), L_END, stream, (const u16*)d_in[0], (const u16*)d_in[1],
                       (const u16*)d_in[2], (const u16*)d_in[3], (const u16*)d_in[4], (const u16*)d_in[5],
                       (const u16*)d_in[7], (const u16*)d_in[8], (u16*)d_out, (char*)d_ws);
}
```

```cpp
#include <hip/hip_runtime.h>
#include <stdint.h>
#include <type_traits>

namespace {
typedef unsigned short u16;
typedef unsigned u32;
typedef u32 v4u __attribute__((ext_vector_type(4)));
typedef u32 v2u __attribute__((ext_vector_type(2)));
typedef float f4 __attribute__((ext_vector_type(4)));
typedef float f2 __attribute__((ext_vector_type(2)));
typedef short s4 __attribute__((ext_vector_type(4)));
typedef __bf16 bf8 __attribute__((ext_vector_type(8)));
typedef __bf16 bf2 __attribute__((ext_vector_type(2)));
#define LAS __attribute__((address_space(3)))
#define GAS __attribute__((address_space(1)))
typedef LAS char lch;
typedef __amdgpu_buffer_rsrc_t rsrc_t;
#define DEV __device__ __forceinline__
#define RLX_AGENT __ATOMIC_RELAXED, __HIP_MEMORY_SCOPE_AGENT

constexpr u32 SD2 = 2048u * 448u * 2u;
constexpr u32 WSTEP = 5504, L_WO = 76 * 1024, L_HALO = L_WO + 28672, L_OWN = L_HALO + 32768, L_CS = L_OWN + 16384,
              L_CNT = L_CS + 384, L_END = L_CNT + 64;
constexpr u32 HQ = 3072, HV = 3840, HK = 4624;
constexpr u32 HSTR = 4160, USTR = 4672;
static_assert(L_END <= 163840 && 14 * WSTEP <= L_WO && 16 * HSTR <= L_WO, "lds map");
constexpr u32 FP = 4352;
constexpr u32 XP = 16640, OP = 14592, KP = 8448, IP = L_HALO + 256;
constexpr u32 WS_F1 = FP, WS_F2 = WS_F1 + 56 * FP, WS_KVT = 0xD0000, WS_YT = WS_KVT + SD2, WS_X2 = WS_YT + SD2,
              WS_O = WS_X2 + 128 * XP, WS_KV = WS_O + 256 * OP, WS_IMG = WS_KV + 1024 * KP, WS_END = WS_IMG + 16 * IP;
static_assert(WS_F2 + 128 * FP <= WS_KVT, "ws map");
constexpr u32 WS_TS = WS_END;
constexpr u32 BIG = 0x7ff00000u;
constexpr u32 SPIN = 1u << 20;

template <class T, class F> DEV T bc(F f) { return __builtin_bit_cast(T, f); }
DEV rsrc_t mkrs(const void* p, u32 bytes) { return __builtin_amdgcn_make_buffer_rsrc((void*)p, 0, bytes, 0x00020000); }
template <int AUX> DEV v4u ld16(rsrc_t r, u32 o) { return __builtin_amdgcn_raw_buffer_load_b128(r, o, 0, AUX); }
template <int AUX> DEV v2u ld8(rsrc_t r, u32 o) { return __builtin_amdgcn_raw_buffer_load_b64(r, o, 0, AUX); }
template <int AUX> DEV u32 ld4(rsrc_t r, u32 o) { return __builtin_amdgcn_raw_buffer_load_b32(r, o, 0, AUX); }
template <int AUX> DEV u32 ld2(rsrc_t r, u32 o) { return (u32)(u16)__builtin_amdgcn_raw_buffer_load_b16(r, o, 0, AUX); }
template <int AUX> DEV void st16(v4u v, rsrc_t r, u32 o) { __builtin_amdgcn_raw_buffer_store_b128(v, r, o, 0, AUX); }
template <int AUX> DEV void st8(v2u v, rsrc_t r, u32 o) { __builtin_amdgcn_raw_buffer_store_b64(v, r, o, 0, AUX); }
template <int AUX> DEV void st4(u32 v, rsrc_t r, u32 o) { __builtin_amdgcn_raw_buffer_store_b32(v, r, o, 0, AUX); }
template <int AUX> DEV v4u ld16s(rsrc_t r, u32 o, u32 so) { return __builtin_amdgcn_raw_buffer_load_b128(r, o, so, AUX); }
template <int AUX> DEV v2u ld8s(rsrc_t r, u32 o, u32 so) { return __builtin_amdgcn_raw_buffer_load_b64(r, o, so, AUX); }
template <int AUX> DEV u32 ld4s(rsrc_t r, u32 o, u32 so) { return __builtin_amdgcn_raw_buffer_load_b32(r, o, so, AUX); }
template <int AUX> DEV void st16s(v4u v, rsrc_t r, u32 o, u32 so) {
    __builtin_amdgcn_raw_buffer_store_b128(v, r, o, so, AUX);
    asm volatile("s_nop 1" ::"v"(v) : "memory");
}
template <int AUX> DEV void st8s(v2u v, rsrc_t r, u32 o, u32 so) { __builtin_amdgcn_raw_buffer_store_b64(v, r, o, so, AUX); }
template <int AUX> DEV void st4s(u32 v, rsrc_t r, u32 o, u32 so) { __builtin_amdgcn_raw_buffer_store_b32(v, r, o, so, AUX); }
template <int AUX> DEV v4u ldk(rsrc_t r, u32 lb, u32 so, int j) { return ld16s<AUX>(r, lb + (j & 3) * 1024, so + (j >> 2) * 4096); }
DEV void sb() { __builtin_amdgcn_sched_barrier(0); }
DEV f2 fm2(f2 a, f2 b, f2 c) { return __builtin_elementwise_fma(a, b, c); }
DEV f4 mma(v4u a, v4u b, f4 c) { return __builtin_amdgcn_mfma_f32_16x16x32_bf16(bc<bf8>(a), bc<bf8>(b), c, 0, 0, 0); }
DEV f4 mmf(float a, float b, f4 c) { return __builtin_amdgcn_mfma_f32_16x16x4f32(a, b, c, 0, 0, 0); }
DEV f4 mma16(v2u a, v2u b, f4 c) { return mma(v4u{a.x, a.y, 0, 0}, v4u{b.x, b.y, 0, 0}, c); }
DEV u32 pk(float lo, float hi) { f2 f = {lo, hi}; return bc<u32>(__builtin_convertvector(f, bf2)); }
DEV float blo(u32 d) { return bc<float>(d << 16); }
DEV float bhi(u32 d) { return bc<float>(d & 0xffff0000u); }
DEV float adj(float hi, u32 lo) { return bc<float>(bc<u32>(__builtin_fabsf(hi) < 1e-30f ? 1e-30f : hi) - lo); }
template <class T> DEV void spin(T& v) { v = __builtin_amdgcn_readfirstlane(v); asm volatile("" : "+s"(v)); }
template <class T> DEV void pin(T& v) { asm volatile("" : "+v"(v)); }
DEV float fm(float a, float b, float c) { return __builtin_fmaf(a, b, c); }
DEV v4u lr16(lch* p) { return *(LAS v4u*)p; }
DEV v2u lr8(lch* p) { return *(LAS v2u*)p; }
DEV u32 lr4(lch* p) { return *(LAS u32*)p; }
DEV void lw16(lch* p, v4u v) { *(LAS v4u*)p = v; }
DEV void lw8(lch* p, v2u v) { *(LAS v2u*)p = v; }
DEV void lw4(lch* p, u32 v) { *(LAS u32*)p = v; }
DEV v2u ltr(lch* p) { return bc<v2u>(__builtin_amdgcn_ds_read_tr16_b64_v4i16((LAS s4*)p)); }
DEV void cbar() { asm volatile("" ::: "memory"); }
DEV void wgbar() { asm volatile("s_waitcnt lgkmcnt(0)\n\ts_barrier" ::: "memory"); }
DEV void drain() { asm volatile("s_waitcnt vmcnt(0)" ::: "memory"); }
DEV float rows_max(float v) {
    auto a = __builtin_amdgcn_permlane32_swap(bc<u32>(v), bc<u32>(v), false, false);
    v = fmaxf(bc<float>(a[0]), bc<float>(a[1]));
    auto b = __builtin_amdgcn_permlane16_swap(bc<u32>(v), bc<u32>(v), false, false);
    return fmaxf(bc<float>(b[0]), bc<float>(b[1]));
}
DEV float rows_sum(float v) {
    auto a = __builtin_amdgcn_permlane32_swap(bc<u32>(v), bc<u32>(v), false, false);
    v = bc<float>(a[0]) + bc<float>(a[1]);
    auto b = __builtin_amdgcn_permlane16_swap(bc<u32>(v), bc<u32>(v), false, false);
    return bc<float>(b[0]) + bc<float>(b[1]);
}
DEV float row3_bcast(float v) {
    auto a = __builtin_amdgcn_permlane32_swap(bc<u32>(v), bc<u32>(v), false, false);
    auto b = __builtin_amdgcn_permlane16_swap(a[1], a[1], false, false);
    return bc<float>(b[1]);
}
#define PSLP 1
DEV void arrive(u32 tag, rsrc_t r, u32 o, u32 so) {
    asm volatile("buffer_atomic_swap %0, %1, %2, %3 offen sc1" ::"v"(tag), "v"(o), "s"(r), "s"(so) : "memory");
}
DEV u32 probe(rsrc_t r, u32 off, u32 so) { return (u32)__builtin_amdgcn_raw_ptr_buffer_atomic_add_i32(0, r, off, so, 16); }
DEV void wait_from(u32 f, rsrc_t r, u32 off, u32 so, u32 tag) {
#pragma unroll 1
    for (u32 it = 0; it < SPIN; ++it) {
        off = f == tag ? BIG : off;
        if (__all(off == BIG)) break;
        __builtin_amdgcn_s_sleep(PSLP);
        f = probe(r, off, so);
    }
    cbar();
}
DEV void wait_flag(rsrc_t r, u32 off, u32 so, u32 tag) { wait_from(probe(r, off, so), r, off, so, tag); }
DEV void wait_lds(lch* p, u32 want) {
#pragma unroll 1
    for (u32 it = 0; it < SPIN * 8; ++it) {
        u32 f = *(volatile LAS u32*)p;
        if (__builtin_amdgcn_readfirstlane(f) >= want) break;
        __builtin_amdgcn_s_sleep(1);
    }
    cbar();
}
DEV void sig_lds(lch* p, int lane) {
    cbar();
    if (lane == 0) asm volatile("ds_add_u32 %0, %1" ::"v"((u32)(uintptr_t)p), "v"(1u) : "memory");
    cbar();
}
#define TSW(n) do { } while (0)
#define TC(n) do { } while (0)
#define TS(n) do { } while (0)

__global__ void __launch_bounds__(512, 2)
k_main(const u16* q, const u16* k, const u16* v, const u16* sk, const u16* hk, const u16* sd,
       const u16* wqkv, const u16* wo, u16* out, char* ws) {
    extern __shared__ __attribute__((aligned(16))) char smem_[];
    lch* const L = (lch*)smem_;
    const int tid = threadIdx.x, lane = tid & 63;
    const int wv = __builtin_amdgcn_readfirstlane(tid >> 6);
    const int w = blockIdx.x, g = w & 7, i = w >> 3, hd = i & 15;
    const int X = wv >> 2, wq = wv & 3, tau = 2 * g + (i >> 4) + 16 * X, b = 4 * tau + wq;
    const int n0 = lane & 15, gq = lane >> 4;
    const u32 lb = lane * 16;
    const u32 l0 = lane == 0 ? 0u : BIG;
    const bool conv = w < 224;
    const int jr = hd < 14 ? hd : 0, jo = jr & 1;
    auto pair_at = [&](int j) { const int p = j + (jr >> 1); return (u32)(p >= 7 ? p - 7 : p); };
    const rsrc_t rws = mkrs(ws, WS_END);
    const v4u ones = {0x3f803f80u, 0x3f803f80u, 0x3f803f80u, 0x3f803f80u};
    const f4 z4 = {0, 0, 0, 0};
    lch* const cnt = L + L_CNT;
    lch* const own = L + L_OWN + X * 8192;
    const u32 img = WS_IMG + (u32)hd * IP;
    const u32 hslot = 24 * (gq >> 1) + ((gq & 1) ? 12 + (n0 < 8 ? n0 ^ 4 : n0) : n0);
    TS(0);
    TC(30);

    v4u xf[14], yf[14];
    float xr, xc;
    u32 tag;
    auto load_q = [&]() {
        const rsrc_t rq = mkrs(q, SD2);
        const u32 o = (u32)((lane >> 3) * 896 + (lane & 7) * 16);
#pragma unroll
        for (int s = 0; s < 14; ++s) xf[s] = ld16s<0>(rq, o, (u32)b * 14336 + (s & 1) * 7168 + pair_at(s >> 1) * 128);
    };
    auto diag_sum = [&](f4 a) {
        const float d = (n0 & 2) ? ((n0 & 1) ? a[3] : a[2]) : ((n0 & 1) ? a[1] : a[0]);
        return rows_sum(gq == (n0 >> 2) ? d : 0.f);
    };
    auto relay_q = [&](lch* sc) {
        lch* pw = sc + ((lane & 7) * 16 + ((lane >> 3) ^ (lane & 7))) * 16;
        const int c0 = gq + 4 * jo, c1 = gq + 4 - 4 * jo;
        lch* pr0 = sc + (c0 * 16 + (n0 ^ c0)) * 16;
        lch* pr1 = sc + (c1 * 16 + (n0 ^ c1)) * 16;
        f4 a1 = z4, a2 = z4;
#pragma unroll
        for (int j = 0; j < 7; ++j) {
            lw16(pw, xf[2 * j]);
            lw16(pw + 128, xf[2 * j + 1]);
            xf[2 * j] = lr16(pr0);
            xf[2 * j + 1] = lr16(pr1);
        }
#pragma unroll
        for (int s = 0; s < 14; ++s) {
            a2 = mma(xf[s], xf[s], a2);
            a1 = mma(ones, xf[s], a1);
        }
        const float s1 = a1[0] * (1.f / 448), s2 = diag_sum(a2) * (1.f / 448);
        xr = __builtin_amdgcn_rsqf(fmaxf(s2 - s1 * s1, 0.f) + 1e-5f);
        xc = -s1 * xr;
        pin(xr);
        pin(xc);
        TS(25);
    };
    if (tid < 16) lw4(cnt + tid * 4, 0);

    if (conv) {
        const int G = w >> 5, tile = w & 31, cch = X, pp = wq;
        v4u kr, vr, kh, h0, h1;
        v2u skw;
        u32 dd;
        {
            const rsrc_t rk = mkrs(k, SD2), rv = mkrs(v, SD2), rh = mkrs(hk, SD2);
            const u32 ro = (u32)((lane >> 3) * 896 + (lane & 7) * 16), so = (u32)((64 * tile + 8 * wv) * 896 + G * 128);
            kr = ld16s<0>(rk, ro, so);
            vr = ld16s<0>(rv, ro, so);
            kh = ld16s<0>(rk, tid < 32 && tile ? ro : BIG, tile ? so - 3584 : 0u);
            skw = ld8s<0>(mkrs(sk, 3584), (u32)(((lane >> 4) & 1) * 32 + (lane & 3) * 8), (u32)(G * 512 + wv * 64));
            const u32 epoch = ld4<16>(rws, 0);
            h0 = ld16s<0>(rh, lb, (u32)(w * 8192 + wv * 1024));
            h1 = ld16<0>(rh, (tid & 255) ? (u32)(w * 8192 + wv * 1024 - 16) + lb : BIG);
            dd = ld4<0>(mkrs(sd, 896), (u32)w * 4);
            if (X == 0) load_q();
            if (tid >= 64 && tid < 128) lw16(L + L_OWN + (lane >> 5) * USTR + 4096 + (lane & 31) * 16, v4u{0, 0, 0, 0});
            if (tid >= 128 && tid < 144) lw16(L + lane * HSTR, v4u{0, 0, 0, 0});
            tag = __builtin_amdgcn_readfirstlane(epoch) + 1u;
            asm volatile("" ::"s"(tag));
        }
        {
            lch* const KS = L + L_WO;
            auto swz = [](int r) { return ((r >> 1) & 1) | (((r >> 3) & 3) << 1); };
            const int r1 = 8 * wv + (lane >> 3), c1 = lane & 7;
            lw16(KS + (r1 + 4) * 128 + ((c1 ^ swz(r1 + 4)) << 4), kr);
            lw16(KS + 8704 + r1 * 128 + ((c1 ^ swz(r1)) << 4), vr);
            if (tid < 32) lw16(KS + r1 * 128 + ((c1 ^ swz(r1)) << 4), kh);
            wgbar();
            TS(24);
            const int r2 = 32 * (lane >> 5) + 8 * (lane & 3) + ((lane >> 2) & 3);
            lch* p0 = KS + r2 * 128 + ((wv ^ swz(r2)) << 4) + ((lane >> 4) & 1) * 8;
            lch* p2 = KS + (r2 + 8) * 128 + ((wv ^ swz(r2 + 8)) << 4) + ((lane >> 4) & 1) * 8;
            const v2u k0 = ltr(p0), k1 = ltr(p0 + 512), k2 = ltr(p2), v0 = ltr(p0 + 8704), v1 = ltr(p0 + 8704 + 512);
            const float kf[11] = {bhi(k0.x), blo(k0.y), bhi(k0.y), blo(k1.x), bhi(k1.x), blo(k1.y),
                                  bhi(k1.y), blo(k2.x), bhi(k2.x), blo(k2.y), bhi(k2.y)};
            const float vf[8] = {blo(v0.x), bhi(v0.x), blo(v0.y), bhi(v0.y), blo(v1.x), bhi(v1.x), blo(v1.y), bhi(v1.y)};
            const float w0 = blo(skw.x), w1 = bhi(skw.x), w2 = blo(skw.y), w3 = bhi(skw.y);
            float r[8];
#pragma unroll
            for (int j = 0; j < 8; ++j) r[j] = fm(kf[j + 3], w0, fm(kf[j + 2], w1, fm(kf[j + 1], w2, kf[j] * w3))) * vf[j];
            st16s<16>(v4u{pk(r[7], r[6]), pk(r[5], r[4]), pk(r[3], r[2]), pk(r[1], r[0])}, rws,
                      (u32)(((lane >> 4) & 1) * 16384 + (lane >> 5) * 256 + n0 * 16),
                      WS_KVT + (u32)((8 * G + wv) * 32768 + tile * 512));
            TS(27);
        }
        {
            const u32 d[8] = {h1.x, h1.y, h1.z, h1.w, h0.x, h0.y, h0.z, h0.w};
            u32 a[7];
#pragma unroll
            for (int j = 0; j < 7; ++j) a[j] = __builtin_amdgcn_alignbit(d[j + 1], d[j], 16);
            lch* p = L + cch * 8 * HSTR + 16 + pp * 1024 + lb;
#pragma unroll
            for (int s = 0; s < 4; ++s) {
                lw16(p + 2 * s * HSTR, v4u{d[4 - s], d[5 - s], d[6 - s], d[7 - s]});
                lw16(p + (2 * s + 1) * HSTR, v4u{a[3 - s], a[4 - s], a[5 - s], a[6 - s]});
            }
        }
        drain();
        arrive(tag, rws, l0, WS_F1 + (u32)(8 * G + wv) * FP + tile * 4);
        TS(1);
        const u32 f1o = lane < 8 ? (u32)lane * 4 : BIG, f1s = WS_F1 + (u32)(w >> 2) * FP + wq * 32;
        u32 f1;
        if (X) load_q();
        else f1 = probe(rws, f1o, f1s);
        wgbar();

        v4u ha[2][8], ub_[2][8], wb[10];
        {
            lch* p = L + (cch * 8 + ((7 - n0) & 7)) * HSTR + ((n0 >> 3) + gq + 8 * pp) * 16;
#pragma unroll
            for (int c = 0; c < 8; ++c)
#pragma unroll
                for (int e = 0; e < 2; ++e) ha[e][c] = lr16(p + c * 512 + e * 64);
        }
        if (X == 0) {
            relay_q(L + L_HALO + wq * 2048);
            wait_from(f1, rws, f1o, f1s, tag);
            TS(2);
            const u32 o = (u32)((lane >> 1) * 64 + (lane & 1) * 16), so = WS_KVT + (u32)((w >> 1) * 16384 + wq * 4096 + (w & 1) * 32);
            const v4u u0 = ld16s<16>(rws, o, so), u1 = ld16s<16>(rws, o + 2048, so);
            lch* p = L + L_OWN + (lane & 1) * USTR + (255 - 32 - 64 * wq - (lane >> 1)) * 16;
            lw16(p + 512, u0);
            lw16(p, u1);
        } else {
            if (wq == 0) {
                wait_flag(rws, lane < 14 ? (u32)lane * 4 : BIG, img + L_HALO, tag);
                sig_lds(cnt + 24, lane);
            } else {
                wait_lds(cnt + 24, 1);
            }
            TS(26);
#pragma unroll
            for (int j = 0; j < 10; ++j) wb[j] = ldk<16>(rws, lb, img + wq * 10240, j);
        }
        lch* const ub = L + L_OWN + cch * USTR + (2032 - 1792 + 64 * pp - 16 * n0 + 8 * gq) * 2;
        lch* const rw = L + L_HALO + wv * 1024 + (gq * 16 + (n0 ^ (gq * 4))) * 16;
        v2u u[2];
        auto finish = [&]() {
            const int col = lane >> 2, qd = lane & 3;
            lch* rr = L + L_HALO + wq * 8192 + (qd * 16 + (col ^ (qd * 4))) * 16;
            f4 p[8];
#pragma unroll
            for (int j = 0; j < 8; ++j) p[j] = bc<f4>(lr16(rr + j * 1024));
            sb();
            f4 y[2];
#pragma unroll
            for (int h = 0; h < 2; ++h) {
                const float d = h ? bhi(dd) : blo(dd);
                y[h] = (p[4 * h] + p[4 * h + 1]) + (p[4 * h + 2] + p[4 * h + 3]);
                y[h][0] = fm(bhi(u[h].y), d, y[h][0]);
                y[h][1] = fm(blo(u[h].y), d, y[h][1]);
                y[h][2] = fm(bhi(u[h].x), d, y[h][2]);
                y[h][3] = fm(blo(u[h].x), d, y[h][3]);
            }
            const u32 a = pk(y[0][0], y[0][1]), c = pk(y[0][2], y[0][3]);
            st16s<16>(v4u{a, c, pk(adj(y[1][0], a & 0xffff), adj(y[1][1], a >> 16)), pk(adj(y[1][2], c & 0xffff), adj(y[1][3], c >> 16))},
                      rws, (u32)(col * 14336 + qd * 16), WS_YT + (u32)(16 * wv * 14336 + w * 64));
        };
        f4 acc[4];
        auto macs = [&](int part, int s0, int s1) {
#pragma unroll
            for (int s = s0; s < s1; ++s)
#pragma unroll
                for (int e = 0; e < 2; ++e)
#pragma unroll
                    for (int t = 0; t < 4; ++t) {
                        const int c = 4 * part + t - s;
                        if (c >= 0) acc[t] = mma(ha[e][c], ub_[e][s], s + e ? acc[t] : z4);
                    }
        };
        wgbar();
        TS(3);
#pragma unroll
        for (int s = 0; s < 4; ++s)
#pragma unroll
            for (int e = 0; e < 2; ++e) ub_[e][s] = lr16(ub + (7 - s) * 512 + e * 64);
#pragma unroll
        for (int h = 0; h < 2; ++h) u[h] = lr8(L + L_OWN + h * USTR + (2044 - 256 * wv - 4 * lane) * 2);
        sb();
        macs(0, 0, 4);
#pragma unroll
        for (int t = 0; t < 4; ++t) lw16(rw + t * 8192, bc<v4u>(acc[t]));
        wgbar();
        if (X == 0) {
            finish();
        } else {
#pragma unroll
            for (int j = 0; j < 10; ++j) lw16(L + wq * 10240 + j * 1024 + lb, wb[j]);
#pragma unroll
            for (int j = 0; j < 9; ++j) wb[j] = ldk<16>(rws, lb, img + 40960 + wq * 9216, j);
        }
        sb();
        macs(1, 0, 3);
#pragma unroll
        for (int t = 0; t < 4; ++t) pin(acc[t]);
#pragma unroll
        for (int s = 4; s < 8; ++s)
#pragma unroll
            for (int e = 0; e < 2; ++e) ub_[e][s] = lr16(ub + (7 - s) * 512 + e * 64);
        if (X == 0) {
            drain();
            arrive(tag, rws, lane < 4 ? (u32)lane * 4 * FP : BIG, WS_F2 + (u32)(wv * 16 + w / 56) * FP + (w % 56) * 4);
            TS(4);
#pragma unroll
            for (int j = 0; j < 7; ++j) wb[j] = ldk<16>(rws, lb, img + L_WO + wq * 7168, j);
        }
        sb();
        macs(1, 3, 8);
#pragma unroll
        for (int t = 0; t < 4; ++t) pin(acc[t]);
        wgbar();
#pragma unroll
        for (int t = 0; t < 4; ++t) lw16(rw + t * 8192, bc<v4u>(acc[t]));
        {
            lch* p = L + (X ? 40960 + wq * 9216 : L_WO + wq * 7168) + lb;
#pragma unroll
            for (int j = 0; j < 7; ++j) lw16(p + j * 1024, wb[j]);
            if (X) {
                lw16(p + 7 * 1024, wb[7]);
                lw16(p + 8 * 1024, wb[8]);
            }
        }
        wgbar();
        if (X) finish();
    } else {
        const int th = (w - 224) >> 1, ts = 7 * (w & 1) + wv, tj = th < 14 ? th : 0;
        const int tp = (ts >> 1) + (tj >> 1), ph = 2 * (tp >= 7 ? tp - 7 : tp) + ((ts ^ tj) & 1);
        v4u wr[8];
        {
            const rsrc_t rq = mkrs(wqkv, wv < 7 ? 448u * 2688u : 0u), ro = mkrs(wo, wv < 7 ? 448u * 896u : 0u);
            const int r = lane >> 2, e = r & 7;
#pragma unroll
            for (int m = 0; m < 6; ++m)
                wr[m] = ld16s<0>(rq, (u32)(r * 2688 + (lane & 3) * 16 + (m >> 1) * 896), (u32)((32 * ph + 16 * (m & 1)) * 2688 + th * 56));
#pragma unroll
            for (int m = 0; m < 2; ++m) {
                const int c = 4 * ts + 2 * m + (r >> 3), h = (c * 43) >> 7;
                const int f = c < 48 ? h * 16 + 4 * c + (e & 3) + (e >> 2) * 16 : (2 * (c - 48) + (e >> 2)) * 28 + 12 + (e & 3);
                wr[6 + m] = ld16s<0>(ro, (u32)(f * 896 + (lane & 3) * 16), (u32)tj * 64);
            }
        }
        tag = __builtin_amdgcn_readfirstlane(ld4<16>(rws, 0)) + 1u;
        asm volatile("" ::"s"(tag));
        wgbar();
        TS(24);
        lch* const sc = L + wv * 8192;
#pragma unroll
        for (int m = 0; m < 8; ++m) lw16(sc + m * 1024 + lb, wr[m]);
        lch* const tr = sc + (8 * gq + (n0 >> 2)) * 64 + (n0 & 3) * 8;
#pragma unroll
        for (int m = 0; m < 8; ++m) {
            lch* p = m < 6 ? tr + (m >> 1) * 2048 + (m & 1) * 32 : tr + 6144 + (n0 & 3) * 8 + (m & 1) * 8;
            const v2u a = ltr(p), c = ltr(p + 256);
            wr[m] = v4u{a.x, a.y, c.x, c.y};
        }
        if (wv < 7) {
            const u32 so = WS_IMG + (u32)th * IP;
            const u32 ho = n0 < 12 ? hslot * 16 : BIG;
            const u32 hk_ = n0 < 12 ? hslot * 16 + 16 : n0 == 12 && gq ? (gq == 1 ? 0u : (u32)(41 + 4 * gq) * 16) : BIG;
#pragma unroll
            for (int m = 0; m < 3; ++m) st16s<16>(wr[2 * m], rws, lb + m * 1024, so + ts * WSTEP);
            st16s<16>(wr[1], rws, ho, so + ts * WSTEP + HQ);
            st16s<16>(n0 < 12 ? wr[3] : ones, rws, hk_, so + ts * WSTEP + HK - 16);
            st16s<16>(wr[5], rws, ho, so + ts * WSTEP + HV);
            st16s<16>(wr[6], rws, lb, so + L_WO + ts * 2048);
            st16s<16>(wr[7], rws, lb + 1024, so + L_WO + ts * 2048);
            TS(27);
            drain();
            arrive(tag, rws, l0, so + L_HALO + ts * 4);
        }
        TS(1);
        load_q();
        if (wv == 7) wait_flag(rws, lane < 14 ? (u32)lane * 4 : BIG, img + L_HALO, tag);
        wgbar();
        TS(26);
        v4u wb[13];
#pragma unroll
        for (int j = 0; j < 13; ++j) wb[j] = ldk<16>(rws, lb, img + wv * 13312, j);
#pragma unroll
        for (int j = 0; j < 13; ++j) lw16(L + wv * 13312 + j * 1024 + lb, wb[j]);
        wgbar();
    }
    lch* const wl = L + lb;
    lch* const wh = L + (n0 < 12 ? (int)hslot : gq < 2 ? -1 : 40 + 4 * gq) * 16;
    if (X) {
        sig_lds(cnt + 20, lane);
        if (wq < 3) {
            f4 c0 = z4, c1 = z4;
            lch* ph = wh + (wq == 0 ? HQ : wq == 1 ? HK : HV);
#pragma unroll
            for (int s = 0; s < 14; ++s) {
                c0 = mma(lr16(wl + wq * 1024 + s * WSTEP), ones, c0);
                c1 = mma(lr16(ph + s * WSTEP), ones, c1);
            }
            if (n0 == 0) {
                lw16(L + L_CS + wq * 128 + gq * 16, bc<v4u>(c0));
                lw16(L + L_CS + wq * 128 + 64 + gq * 16, bc<v4u>(c1));
            }
            sig_lds(cnt + 28, lane);
        }
        if (conv) {
            drain();
            arrive(tag, rws, lane < 4 ? (u32)lane * 4 * FP : BIG, WS_F2 + (u32)(wv * 16 + w / 56) * FP + (w % 56) * 4);
            TS(4);
        }
    }
    if (X || !conv) relay_q(L + L_OWN + wv * 2048);
    TS(5);

#define IDS()                                                                                              \
    int ln_ = lane0, wv_ = wv0, w_ = w0;                                                                   \
    asm volatile("" : "+v"(ln_)); spin(wv_); spin(w_);                                                     \
    const int lane = ln_, wv = wv_, w = w_, hd = (w >> 3) & 15, X = wv >> 2, wq = wv & 3;                  \
    const int tau = 2 * (w & 7) + (w >> 7) + 16 * X, b = 4 * tau + wq, n0 = lane & 15, gq = lane >> 4;     \
    const u32 lb = lane * 16;                                                                              \
    (void)hd, (void)b, (void)n0, (void)gq, (void)lb
    const int lane0 = lane, wv0 = wv, w0 = w;
    u32 kvo = WS_KV + (u32)(hd * 32 + tau) * KP + wq * 2048, fo, hs, f;
    v4u qf, res;
    f4 ck0, ck1;
    float bv0, bv1;
    v4u wf[2][4];
    auto ldw = [&](int s, int j) {
        wf[s & 1][j] = lr16((j == 0 ? wh + HK : j == 3 ? wh + HV : wl + j * 1024) + s * WSTEP);
    };
    auto operands = [&]() {
#pragma unroll
        for (int j = 0; j < 4; ++j) ldw(0, j);
        ck0 = bc<f4>(lr16(L + L_CS + 128 + gq * 16));
        ck1 = bc<f4>(lr16(L + L_CS + 192 + gq * 16));
        const float m = gq == 3 ? -1.f / 448 : 0.f;
        bv0 = bc<float>(lr4(L + L_CS + 256 + n0 * 4)) * m;
        bv1 = bc<float>(lr4(L + L_CS + 320 + (n0 < 12 ? n0 : 0) * 4)) * m;
        pin(bv0);
        pin(bv1);
    };
    auto kvproj = [&](auto first_) {
        constexpr bool FIRST = decltype(first_)::value;
        lch* sc = own + (wq >> 1) * 4096 + (wq & 1) * 1024;
        lch* tr = sc + gq * 256 + (n0 >> 3) * 64 + (n0 & 3) * 16 + ((n0 >> 2) & 1) * 8;
        auto ytr = [&](int s) {
            lw16(sc + lb, yf[s]);
            const v2u a = ltr(tr), c = ltr(tr + 128);
            yf[s] = v4u{a.x, a.y, c.x, c.y};
        };
        f4 ak0, ak1, av0, av1, ag;
        float xl[4], xh[4], yl[4];
        auto unp = [&](int s, int j) {
            xl[j] = blo(xf[s][j]);
            yl[j] = blo(yf[s][j]);
            pin(xl[j]);
            pin(yl[j]);
            if (FIRST) {
                xh[j] = bhi(xf[s][j]);
                pin(xh[j]);
            }
        };
        auto gate = [&](int s) {
            const f2 r2 = {xr, xr}, c2 = {xc, xc};
            f2 x[4], y[4], z[4];
#pragma unroll
            for (int h = 0; h < 2; ++h) {
                x[h] = f2{xl[2 * h], xl[2 * h + 1]};
                y[h] = f2{yl[2 * h], yl[2 * h + 1]};
                x[2 + h] = FIRST ? f2{xh[2 * h], xh[2 * h + 1]} : f2{bc<float>(xf[s][2 * h]), bc<float>(xf[s][2 * h + 1])};
                y[2 + h] = f2{bc<float>(yf[s][2 * h]), bc<float>(yf[s][2 * h + 1])};
            }
#pragma unroll
            for (int h = 0; h < 4; ++h) z[h] = fm2(x[h], r2, c2);
            sb();
#pragma unroll
            for (int h = 0; h < 4; ++h) z[h] = fm2(z[h], y[h], x[h]);
            sb();
            return v4u{pk(z[0].x, z[2].x), pk(z[0].y, z[2].y), pk(z[1].x, z[3].x), pk(z[1].y, z[3].y)};
        };
        if (FIRST) {
            ytr(0);
            ytr(1);
        }
#pragma unroll
        for (int j = 0; j < 4; ++j) unp(0, j);
        sb();
#pragma unroll
        for (int s = 0; s < 14; ++s) {
            xf[s] = gate(s);
            pin(xf[s]);
            sb();
            auto slot = [&](int j) {
                if (s < 13) {
                    ldw(s + 1, j);
                    unp(s + 1, j);
                }
                sb();
            };
            ak1 = mma(wf[s & 1][0], xf[s], s ? ak1 : z4);
            slot(0);
            ag = mma(xf[s], xf[s], s ? ag : z4);
            slot(1);
            ak0 = mma(wf[s & 1][1], xf[s], s ? ak0 : z4);
            slot(2);
            av0 = mma(xf[s], wf[s & 1][2], s ? av0 : z4);
            slot(3);
            av1 = mma(xf[s], wf[s & 1][3], s ? av1 : z4);
            if (FIRST && s < 12) ytr(s + 2);
            sb();
        }
        constexpr int ly = FIRST ? 0 : 1;
        TS(8 + 8 * ly);
        v4u kf, vf;
        float nm, r;
        {
            av0 = mmf(ak1[0], bv0, av0);
            av1 = mmf(ak1[0], bv1, av1);
            nm = row3_bcast(ak1[0]) * (-1.f / 448);
            r = __builtin_amdgcn_rsqf(fmaxf(fm(-nm, nm, diag_sum(ag) * (1.f / 448)), 0.f) + 1e-5f);
            const f4 rv = mmf(r, gq == 3 ? 1.f : 0.f, z4);
            float t0[4], t1[4];
#pragma unroll
            for (int e = 0; e < 4; ++e) { t0[e] = fm(nm, ck0[e], ak0[e]) * r; t1[e] = fm(nm, ck1[e], ak1[e]) * r; }
            kf = v4u{pk(t0[0], t0[1]), pk(t0[2], t0[3]), gq == 3 ? 0u : pk(t1[0], t1[1]), gq == 3 ? 0u : pk(t1[2], t1[3])};
            st16s<16>(kf, rws, lb, kvo);
            av0 *= rv;
            av1 *= rv;
            vf = v4u{pk(av0[0], av0[1]), pk(av0[2], av0[3]), n0 >= 12 ? 0x3f803f80u : pk(av1[0], av1[1]),
                     n0 >= 12 ? 0x3f803f80u : pk(av1[2], av1[3])};
            st16s<16>(vf, rws, lb, kvo + 1024);
            sb();
            TS(36 + 8 * ly);
        }
        {
            IDS();
            lch* const own = L + L_OWN + X * 8192;
            v4u qw[8];
            auto ldq = [&](int j) { qw[j & 7] = lr16(((j & 1) ? wh + HQ : wl) + (j >> 1) * WSTEP); };
            if (ly) wait_lds(cnt + 16 + X * 4, 4 + 4 * X);
#pragma unroll
            for (int j = 0; j < 8; ++j) ldq(j);
            lch* ob = own + (wq >> 1) * 4096 + lb;
            lw16(ob + (wq & 1) * 1024, kf);
            lw8(ob + 2048 + (wq & 1) * 8, v2u{vf.x, vf.y});
            lw8(ob + 3072 + (wq & 1) * 8, v2u{vf.z, vf.w});
            const f4 cq0 = bc<f4>(lr16(L + L_CS + gq * 16)), cq1 = bc<f4>(lr16(L + L_CS + 64 + gq * 16));
            const bool real = tau - 4 + wq >= 0;
            fo = lane < 4 && real ? (u32)lane * 4 : BIG;
            u32 ao_ = lane ? BIG : 0u;
            u32 as = kvo + 8192 - (u32)wq * 2044;
            hs = real ? kvo - (u32)(4 - wq) * KP - (u32)wq * 2048 : 0u;
            asm volatile("" : "+v"(fo), "+v"(ao_));
            spin(as); spin(hs);
            sb();
            f4 aq[2];
#pragma unroll
            for (int j = 0; j < 28; ++j) {
                aq[j & 1] = mma(qw[j & 7], xf[j >> 1], j > 1 ? aq[j & 1] : z4);
                if (j + 8 < 28) ldq(j + 8);
                sb();
            }
            TS(37 + 8 * ly);
            drain();
            arrive(tag, rws, ao_, as);
            f = probe(rws, fo, hs + 8192);
            sb();
            TS(9 + 8 * ly);
            const float rq = r * (0.18898223650461362f * 1.4426950408889634f);
            float t0[4], t1[4];
#pragma unroll
            for (int e = 0; e < 4; ++e) { t0[e] = fm(nm, cq0[e], aq[0][e]) * rq; t1[e] = fm(nm, cq1[e], aq[1][e]) * rq; }
            qf = v4u{pk(t0[0], t0[1]), pk(t0[2], t0[3]), gq == 3 ? 0x3f80u : pk(t1[0], t1[1]), gq == 3 ? 0u : pk(t1[2], t1[3])};
            pin(qf);
            res = xf[0];
        }
    };

    {
        const u32 lbe = lb + jo * 1024, lbo = lb + 1024 - jo * 1024;
        wait_flag(rws, lane < 56 ? (u32)lane * 4 : BIG, WS_F2 + (u32)b * FP, tag);
        sig_lds(cnt + X * 4, lane);
        wait_lds(cnt + X * 4, 4);
        TS(6);
        const u32 so = WS_YT + (u32)b * 14336;
#pragma unroll
        for (int s = 0; s < 14; ++s) {
            yf[s] = ld16s<16>(rws, (s & 1) ? lbo : lbe, so + pair_at(s >> 1) * 2048);
            sb();
        }
        if (w == 0 && wv == 0) st4<16>(tag, rws, l0);
        wait_lds(cnt + 28, 3);
        operands();
        spin(kvo);
        sb();
        TSW(35);
        kvproj(std::true_type{});
    }
#pragma unroll 1
    for (int ly = 0;; ++ly) {
        u32 oo;
        {
            IDS();
            lch* const own = L + L_OWN + X * 8192;
            const bool real = tau - 4 + wq >= 0;
            const int P0 = (wq + 1) >> 1, par = wq & 1;
            auto blk = [&](int P) { return P < 8 ? L + L_HALO + P * 4096 + lb : own + (P - 8) * 4096 + lb; };
            lch* a0 = blk(P0);
            lch* a6 = blk(P0 + 6);
            lch* a7 = blk(P0 + 7);
            lch* ak = blk(par ? wq >> 1 : 8 + (wq >> 1)) + par * 1024;
            lch* av = blk(par ? wq >> 1 : 8 + (wq >> 1)) + par * 8;
            lch* hp = L + L_HALO + wq * 8192 + lb;
            const int dl = n0 - 4 * gq;
            f4 mf, ml, mo;
#pragma unroll
            for (int e = 0; e < 4; ++e) {
                const bool gt = e > dl;
                mf[e] = (par || gt) ? 0.f : -1e30f;
                ml[e] = (!par || !gt) ? 0.f : -1e30f;
                mo[e] = (gt == (bool)par) ? 0.f : -1e30f;
            }
            u32 kpad = !real && gq == 3 ? 0xf149u : 0u, hl = real ? lb : BIG;
            u32 so0 = gq < 3 ? lb : BIG, so1 = gq == 3 ? (u32)n0 * 8 : BIG;
            oo = WS_O + (u32)(ly * 128 + b) * OP;
            u32 os0 = oo + hd * 768, os1 = oo + 12288 + hd * 128;
            lch* c_kv = cnt + 8 + X * 4;
            asm volatile("" : "+v"(mf), "+v"(ml), "+v"(mo), "+v"(a0), "+v"(a6), "+v"(a7), "+v"(ak), "+v"(av), "+v"(hp), "+v"(kpad), "+v"(so0), "+v"(so1), "+v"(c_kv));
            spin(os0); spin(os1); spin(oo);
            wait_lds(cnt + 16 + (X ^ 1) * 4, 4 * (ly + 1));
            wait_from(f, rws, fo, hs + 8192, tag);
            TS(10 + 8 * ly);
            pin(hl);
            v4u hb[8];
#pragma unroll
            for (int j = 0; j < 8; ++j) hb[j] = ldk<16>(rws, hl, hs, j);
            TSW(38 + 8 * ly);
#pragma unroll
            for (int j = 0; j < 4; ++j) {
                lch* p = hp + (j >> 1) * 4096;
                hb[2 * j].z |= kpad;
                lw16(p + (j & 1) * 1024, hb[2 * j]);
                lw8(p + 2048 + (j & 1) * 8, v2u{hb[2 * j + 1].x, hb[2 * j + 1].y});
                lw8(p + 3072 + (j & 1) * 8, v2u{hb[2 * j + 1].z, hb[2 * j + 1].w});
            }
            sig_lds(c_kv, lane);
            wait_lds(c_kv, 4 * (ly + 1));
            TS(11 + 8 * ly);
            v4u kf[17];
            kf[16] = lr16(ak);
#pragma unroll
            for (int u = 0; u < 16; ++u) kf[u] = lr16((u < 12 ? a0 + (u >> 1) * 4096 : u < 14 ? a6 : a7) + (u & 1) * 1024);
            sb();
            f4 s[17];
            s[16] = mma(kf[16], qf, mo);
#pragma unroll
            for (int u = 0; u < 16; ++u) s[u] = mma(kf[u], qf, u == 0 ? mf : u == 15 ? ml : f4{0, 0, 0, 0});
            v2u vo0 = lr8(av + 2048), vo1 = lr8(av + 3072);
            v4u vf[8][2];
#pragma unroll
            for (int u = 0; u < 8; ++u) {
                lch* p = u < 6 ? a0 + u * 4096 : u == 6 ? a6 : a7;
                vf[u][0] = lr16(p + 2048);
                vf[u][1] = lr16(p + 3072);
            }
            float mx = fmaxf(fmaxf(s[16][0], s[16][1]), fmaxf(s[16][2], s[16][3]));
#pragma unroll
            for (int u = 0; u < 16; ++u) mx = fmaxf(fmaxf(fmaxf(fmaxf(mx, s[u][0]), s[u][1]), s[u][2]), s[u][3]);
#pragma unroll
            for (int j = 0; j < 17; ++j) {
                __builtin_amdgcn_sched_group_barrier(0x8, 1, 0);
                __builtin_amdgcn_sched_group_barrier(0x100, 1, 0);
                if (j >= 2) __builtin_amdgcn_sched_group_barrier(0x2, 2, 0);
            }
            sb();
            sig_lds(c_kv + 8, lane);
            mx = rows_max(mx);
            auto pe = [&](int u) {
                return v2u{pk(__builtin_amdgcn_exp2f(s[u][0] - mx), __builtin_amdgcn_exp2f(s[u][1] - mx)),
                           pk(__builtin_amdgcn_exp2f(s[u][2] - mx), __builtin_amdgcn_exp2f(s[u][3] - mx))};
            };
            f4 o0 = {0, 0, 0, 0}, o1 = o0;
            {
                const v2u p = pe(16);
                o0 = mma16(vo0, p, o0);
                o1 = mma16(vo1, p, o1);
            }
#pragma unroll
            for (int u = 0; u < 8; ++u) {
                const v2u pa = pe(2 * u), pb = pe(2 * u + 1);
                const v4u pf = {pa.x, pa.y, pb.x, pb.y};
                o0 = mma(vf[u][0], pf, o0);
                o1 = mma(vf[u][1], pf, o1);
            }
            const float inv = __builtin_amdgcn_rcpf(row3_bcast(o1[0]));
            o0 *= inv;
            o1 *= inv;
            const v4u of = {pk(o0[0], o0[1]), pk(o0[2], o0[3]), pk(o1[0], o1[1]), pk(o1[2], o1[3])};
            st16s<16>(of, rws, so0, os0);
            st8s<16>(v2u{of.x, of.y}, rws, so1, os1);
            sb();
            TS(12 + 8 * ly);
        }
        {
            IDS();
            const u32 xo_ = WS_X2 + (u32)b * XP;
            lch* wp = L + L_WO + lb;
            v4u wg[28];
#pragma unroll
            for (int j = 0; j < 16; ++j) wg[j] = lr16(wp + j * 1024);
            f4 c0 = {blo(res.x), bhi(res.x), blo(res.y), bhi(res.y)}, c1 = {blo(res.z), bhi(res.z), blo(res.w), bhi(res.w)};
            u32 fo = lane < 16 ? (u32)lane * 4 : BIG, ao_ = lane ? BIG : (u32)hd * 4, lo = gq * 256 + n0 * 8, lb2 = lb;
            u32 st = gq == 0 ? (u32)n0 * 8 : BIG, uo = n0 * 896 + gq * 16;
            u32 xs = xo_ + hd * 1024, ss = xo_ + 14336 + hd * 128, us = b * 14336 + hd * 64;
            asm volatile("" : "+v"(fo), "+v"(ao_), "+v"(lo), "+v"(c0), "+v"(c1), "+v"(st), "+v"(uo));
            spin(xs); spin(ss); spin(us);
            drain();
            arrive(tag, rws, ao_, oo + 14336);
            TS(13 + 8 * ly);
            TC(31);
            if (hd < 14) {
                wait_flag(rws, fo, oo + 14336, tag);
                TS(14 + 8 * ly);
                pin(lb2);
                pin(lo);
                v4u of[14];
#pragma unroll
                for (int s = 0; s < 12; ++s) of[s] = ldk<16>(rws, lb2, oo, s);
#pragma unroll
                for (int s = 12; s < 14; ++s) {
                    const v2u a = ld8s<16>(rws, lo + (s - 12) * 1024, oo + 12288);
                    const v2u c = ld8s<16>(rws, lo + (s - 12) * 1024 + 128, oo + 12288);
                    of[s] = v4u{a.x, a.y, c.x, c.y};
                }
                sb();
                TSW(32 + 8 * ly);
#pragma unroll
                for (int s = 0; s < 14; ++s) {
                    c0 = mma(wg[2 * s], of[s], c0);
                    if (2 * s + 16 < 28) wg[2 * s + 16] = lr16(wp + (2 * s + 16) * 1024);
                    c1 = mma(wg[2 * s + 1], of[s], c1);
                    if (2 * s + 17 < 28) wg[2 * s + 17] = lr16(wp + (2 * s + 17) * 1024);
                    sb();
                }
                TS(33 + 8 * ly);
                if (ly == 0) {
                    const u32 e0 = pk(c0[0], c0[2]), e1 = pk(c1[0], c1[2]);
                    st16s<16>(v4u{pk(c0[0], adj(c0[1], e0 & 0xffff)), pk(c0[2], adj(c0[3], e0 >> 16)),
                                  pk(c1[0], adj(c1[1], e1 & 0xffff)), pk(c1[2], adj(c1[3], e1 >> 16))},
                              rws, lb2, xs);
                    const f4 a1 = c0 + c1, a2 = c0 * c0 + c1 * c1;
                    const float s1 = rows_sum((a1[0] + a1[1]) + (a1[2] + a1[3]));
                    const float s2 = rows_sum((a2[0] + a2[1]) + (a2[2] + a2[3]));
                    st8s<16>(v2u{bc<u32>(s1), bc<u32>(s2)}, rws, st, ss);
                    TS(34);
                } else {
                    st16s<0>(v4u{pk(c0[0], c0[1]), pk(c0[2], c0[3]), pk(c1[0], c1[1]), pk(c1[2], c1[3])}, mkrs(out, SD2), uo, us);
                    TS(23);
                }
            }
        }
        if (ly) break;
        {
            IDS();
            const u32 xo_ = WS_X2 + (u32)b * XP;
            const int jr = hd < 14 ? hd : 0, jo = jr & 1;
            u32 lbe = lb + jo * 1024, lbo = lb + 1024 - jo * 1024, ps[7];
#pragma unroll
            for (int j = 0; j < 7; ++j) {
                const int p = j + (jr >> 1);
                ps[j] = xo_ + (p >= 7 ? p - 7 : p) * 2048;
                spin(ps[j]);
            }
            u32 fo = lane < 14 ? (u32)lane * 4 : BIG, ao_ = lane || hd >= 14 ? BIG : (u32)hd * 4;
            u32 l8 = lane * 8, l8m = gq < 2 ? l8 + 1536 : BIG;
            kvo += 512 * KP;
            operands();
            asm volatile("" : "+v"(fo), "+v"(ao_), "+v"(lbe), "+v"(lbo), "+v"(l8m));
            spin(kvo);
            drain();
            arrive(tag, rws, ao_, xo_ + 16128);
            TS(15);
            wait_flag(rws, fo, xo_ + 16128, tag);
            TS(7);
            pin(l8);
            v2u sp[4];
#pragma unroll
            for (int m = 0; m < 4; ++m) sp[m] = ld8s<16>(rws, m < 3 ? l8 + m * 512 : l8m, xo_ + 14336);
            sb();
#pragma unroll
            for (int s = 0; s < 14; ++s) {
                xf[s] = ld16s<16>(rws, (s & 1) ? lbo : lbe, ps[s >> 1]);
                sb();
            }
            TSW(43);
            float s1 = (bc<float>(sp[0].x) + bc<float>(sp[1].x)) + (bc<float>(sp[2].x) + bc<float>(sp[3].x));
            float s2 = (bc<float>(sp[0].y) + bc<float>(sp[1].y)) + (bc<float>(sp[2].y) + bc<float>(sp[3].y));
            s1 = rows_sum(s1) * (1.f / 448);
            s2 = rows_sum(s2) * (1.f / 448);
            xr = __builtin_amdgcn_rsqf(fmaxf(s2 - s1 * s1, 0.f) + 1e-5f);
            xc = -s1 * xr;
#pragma unroll
            for (int s = 0; s < 14; ++s) pin(yf[s]);
            kvproj(std::false_type{});
        }
    }
}
}

extern "C" void kernel_launch(void* const* d_in, const int* in_sizes, int n_in, void* d_out, int out_size,
                              void* d_ws, size_t ws_size, hipStream_t stream) {
    (void)in_sizes; (void)n_in; (void)out_size; (void)ws_size;
    (void)hipFuncSetAttribute((const void*)k_main, hipFuncAttributeMaxDynamicSharedMemorySize, (int)L_END);
    hipLaunchKernelGGL(k_main, dim3(256), dim3(512), L_END, stream, (const u16*)d_in[0], (const u16*)d_in[1],
                       (const u16*)d_in[2], (const u16*)d_in[3], (const u16*)d_in[4], (const u16*)d_in[5],
                       (const u16*)d_in[7], (const u16*)d_in[8], (u16*)d_out, (char*)d_ws);
}
```

```cpp
#include <hip/hip_runtime.h>
#include <stdint.h>
#include <type_traits>

extern "C" __device__ int buf_swap_(int, __amdgpu_buffer_rsrc_t, int, int, int) __asm("llvm.amdgcn.raw.ptr.buffer.atomic.swap.i32");

namespace {
typedef unsigned short u16;
typedef unsigned u32;
typedef u32 v4u __attribute__((ext_vector_type(4)));
typedef u32 v2u __attribute__((ext_vector_type(2)));
typedef float f4 __attribute__((ext_vector_type(4)));
typedef float f2 __attribute__((ext_vector_type(2)));
typedef short s4 __attribute__((ext_vector_type(4)));
typedef __bf16 bf8 __attribute__((ext_vector_type(8)));
typedef __bf16 bf2 __attribute__((ext_vector_type(2)));
#define LAS __attribute__((address_space(3)))
typedef LAS char lch;
typedef __amdgpu_buffer_rsrc_t rsrc_t;
#define DEV __device__ __forceinline__

constexpr u32 SD2 = 2048u * 448u * 2u;
constexpr u32 WSTEP = 5440, OSTEP = 1792;
constexpr u32 HQ = 3072, HV = 3840, HK = 4608;
constexpr u32 L_WO = 14 * WSTEP, L_SCR = L_WO + 16 * OSTEP, L_OWNB = L_SCR + 8192, L_HALO = L_OWNB + 8192,
              L_OWNA = L_HALO + 32768, L_CS = L_OWNA + 8192, L_CNT = L_CS + 384, L_END = L_CNT + 128;
constexpr u32 HSTR = 4160, USTR = 40960 + 64;
static_assert(L_END <= 163840 && 16 * HSTR <= L_WO && 104 * 1024 <= L_OWNB, "lds map");
constexpr u32 FP = 4352;
constexpr u32 XP = 17664, SP = 5376, KP = 8448, IP = 104 * 1024 + 256;
constexpr u32 WS_F1 = FP, WS_F2 = WS_F1 + 56 * FP, WS_ST = 0xC8000, WS_KVT = 0xD0000, WS_QT = WS_KVT + SD2, WS_X1 = WS_QT + SD2,
              WS_YT = WS_X1 + SD2, WS_X3 = WS_YT + SD2, WS_O = WS_X3 + 128 * XP, WS_S1 = WS_O + 256 * XP,
              WS_KV = WS_S1 + 128 * SP, WS_IMG = WS_KV + 1024 * KP, WS_END = WS_IMG + 16 * IP;
static_assert(WS_F2 + 128 * FP <= WS_ST, "ws map");
constexpr u32 WS_TS = WS_END;
constexpr u32 BIG = 0x7ff00000u;
constexpr u32 SPIN = 1u << 15;

template <class T, class F> DEV T bc(F f) { return __builtin_bit_cast(T, f); }
DEV rsrc_t mkrs(const void* p, u32 bytes) { return __builtin_amdgcn_make_buffer_rsrc((void*)p, 0, bytes, 0x00020000); }
template <int AUX> DEV v4u ld16(rsrc_t r, u32 o) { return __builtin_amdgcn_raw_buffer_load_b128(r, o, 0, AUX); }
template <int AUX> DEV u32 ld4(rsrc_t r, u32 o) { return __builtin_amdgcn_raw_buffer_load_b32(r, o, 0, AUX); }
template <int AUX> DEV void st4(u32 v, rsrc_t r, u32 o) { __builtin_amdgcn_raw_buffer_store_b32(v, r, o, 0, AUX); }
template <int AUX> DEV v4u ld16s(rsrc_t r, u32 o, u32 so) { return __builtin_amdgcn_raw_buffer_load_b128(r, o, so, AUX); }
template <int AUX> DEV v2u ld8s(rsrc_t r, u32 o, u32 so) { return __builtin_amdgcn_raw_buffer_load_b64(r, o, so, AUX); }
template <int AUX> DEV void st16s(v4u v, rsrc_t r, u32 o, u32 so) {
    __builtin_amdgcn_raw_buffer_store_b128(v, r, o, so, AUX);
    asm volatile("s_nop 1" ::"v"(v) : "memory");
}
template <int AUX> DEV void st8s(v2u v, rsrc_t r, u32 o, u32 so) { __builtin_amdgcn_raw_buffer_store_b64(v, r, o, so, AUX); }
template <int AUX> DEV v4u ldk(rsrc_t r, u32 lb, u32 so, int j) { return ld16s<AUX>(r, lb + (j & 3) * 1024, so + (j >> 2) * 4096); }
DEV void sb() { __builtin_amdgcn_sched_barrier(0); }
DEV f4 mma(v4u a, v4u b, f4 c) { return __builtin_amdgcn_mfma_f32_16x16x32_bf16(bc<bf8>(a), bc<bf8>(b), c, 0, 0, 0); }
DEV u32 pk(float lo, float hi) { f2 f = {lo, hi}; return bc<u32>(__builtin_convertvector(f, bf2)); }
DEV float blo(u32 d) { return bc<float>(d << 16); }
DEV float bhi(u32 d) { return bc<float>(d & 0xffff0000u); }
DEV float dot2(u32 a, u32 b, float c) { return __builtin_amdgcn_fdot2_f32_bf16(bc<bf2>(a), bc<bf2>(b), c, false); }
DEV u32 xor_or(u32 a, u32 b, u32 c) { return __builtin_amdgcn_bitop3_b32(a, b, c, 0xbe); }
template <class T> DEV void spin(T& v) { v = __builtin_amdgcn_readfirstlane(v); asm volatile("" : "+s"(v)); }
template <class T> DEV void pin(T& v) { asm volatile("" : "+v"(v)); }
DEV float fm(float a, float b, float c) { return __builtin_fmaf(a, b, c); }
DEV v4u lr16(lch* p) { return *(LAS v4u*)p; }
DEV v2u lr8(lch* p) { return *(LAS v2u*)p; }
DEV void lw16(lch* p, v4u v) { *(LAS v4u*)p = v; }
DEV void lw8(lch* p, v2u v) { *(LAS v2u*)p = v; }
DEV void lw4(lch* p, u32 v) { *(LAS u32*)p = v; }
DEV v2u ltr(lch* p) { return bc<v2u>(__builtin_amdgcn_ds_read_tr16_b64_v4i16((LAS s4*)p)); }
DEV v4u cat(v2u a, v2u b) { return v4u{a.x, a.y, b.x, b.y}; }
DEV void cbar() { asm volatile("" ::: "memory"); }
DEV void wgbar() { asm volatile("s_waitcnt lgkmcnt(0)\n\ts_barrier" ::: "memory"); }
DEV void drain() { asm volatile("s_waitcnt vmcnt(0)" ::: "memory"); }
DEV float rows_max(float v) {
    auto a = __builtin_amdgcn_permlane32_swap(bc<u32>(v), bc<u32>(v), false, false);
    v = fmaxf(bc<float>(a[0]), bc<float>(a[1]));
    auto b = __builtin_amdgcn_permlane16_swap(bc<u32>(v), bc<u32>(v), false, false);
    return fmaxf(bc<float>(b[0]), bc<float>(b[1]));
}
DEV float rows_sum(float v) {
    auto a = __builtin_amdgcn_permlane32_swap(bc<u32>(v), bc<u32>(v), false, false);
    v = bc<float>(a[0]) + bc<float>(a[1]);
    auto b = __builtin_amdgcn_permlane16_swap(bc<u32>(v), bc<u32>(v), false, false);
    return bc<float>(b[0]) + bc<float>(b[1]);
}
template <int R> DEV float ror_add(float v) { return v + bc<float>(__builtin_amdgcn_update_dpp(0u, bc<u32>(v), 0x120 + R, 0xf, 0xf, false)); }
DEV float wave_sum(float v) { return rows_sum(ror_add<1>(ror_add<2>(ror_add<4>(ror_add<8>(v))))); }
DEV void arrive(u32 tag, rsrc_t r, u32 o, u32 so) { (void)buf_swap_((int)tag, r, (int)o, (int)so, 16); }
DEV u32 probe(rsrc_t r, u32 off, u32 so) { return (u32)__builtin_amdgcn_raw_ptr_buffer_atomic_add_i32(0, r, off, so, 16); }
DEV void wait_from(u32 f, rsrc_t r, u32 off, u32 so, u32 tag) {
#pragma unroll 1
    for (u32 it = 0; it < SPIN; ++it) {
        off = f == tag ? BIG : off;
        if (__all(off == BIG)) break;
        __builtin_amdgcn_s_sleep(1);
        f = probe(r, off, so);
    }
    cbar();
}
DEV void wait_flag(rsrc_t r, u32 off, u32 so, u32 tag) { wait_from(probe(r, off, so), r, off, so, tag); }
template <int N> DEV void wait3(u32& f0, u32& f1, u32& f2, rsrc_t r, u32 off, u32 so, u32 tag) {
    const unsigned long long msk = __ballot(off != BIG);
    u32 cnt = SPIN;
#define CHK(F) "v_cmp_ne_u32 vcc, %[tag], %[" F "]\n\ts_and_b64 vcc, vcc, %[msk]\n\ts_cbranch_scc0 2f\n\tv_mov_b32 %[" F "], 0\n\t" \
               "buffer_atomic_add %[" F "], %[off], %[rs], %[so] offen sc0 sc1\n\t"
    asm volatile("s_waitcnt vmcnt(%[n])\n\t" CHK("f0") "s_waitcnt vmcnt(%[n])\n\t" CHK("f1")
                 "1:\n\ts_waitcnt vmcnt(2)\n\t" CHK("f2") "s_waitcnt vmcnt(2)\n\t" CHK("f0") "s_waitcnt vmcnt(2)\n\t" CHK("f1")
                 "s_sub_u32 %[cnt], %[cnt], 1\n\ts_cbranch_scc0 1b\n2:"
                 : [f0] "+v"(f0), [f1] "+v"(f1), [f2] "+v"(f2), [cnt] "+s"(cnt)
                 : [tag] "s"(tag), [msk] "s"(msk), [off] "v"(off), [rs] "s"(r), [so] "s"(so), [n] "n"(N + 2)
                 : "memory", "vcc", "scc");
#undef CHK
}
DEV void probe_(u32& f, rsrc_t r, u32 off, u32 so) {
    asm volatile("v_mov_b32 %0, 0\n\tbuffer_atomic_add %0, %1, %2, %3 offen sc0 sc1" : "=&v"(f) : "v"(off), "s"(r), "s"(so) : "memory");
}
DEV void keep(u32 a, u32 b, u32 c) { asm volatile("" ::"v"(a), "v"(b), "v"(c)); }
DEV void wait_lds(lch* p, u32 want) {
#pragma unroll 1
    for (u32 it = 0; it < SPIN * 8; ++it) {
        u32 f = *(volatile LAS u32*)p;
        if (__builtin_amdgcn_readfirstlane(f) >= want) break;
        __builtin_amdgcn_s_sleep(1);
    }
    cbar();
}
DEV void sig_lds(lch* p, int lane) {
    cbar();
    if (lane == 0) asm volatile("ds_add_u32 %0, %1" ::"v"((u32)(uintptr_t)p), "v"(1u) : "memory");
    cbar();
}
#define TN(n, v) do { } while (0)
#define TC(n) do { } while (0)
#define TS(n) do { } while (0)
__global__ void __launch_bounds__(512, 2)
k_main(const u16* q, const u16* k, const u16* v, const u16* sk, const u16* hk, const u16* sd,
       const u16* wqkv, const u16* wo, u16* out, char* ws) {
    extern __shared__ __attribute__((aligned(16))) char smem_[];
    lch* const L = (lch*)smem_;
    const int tid = threadIdx.x, lane = tid & 63;
    const int wv = __builtin_amdgcn_readfirstlane(tid >> 6);
    const int w = blockIdx.x, hd = (w >> 3) & 15, pi = 2 * (w & 7) + (w >> 7);
    const int X = wv >> 2, wq = wv & 3, tau = pi + 16 * X, b = 4 * tau + wq;
    const int n0 = lane & 15, gq = lane >> 4;
    const u32 lb = lane * 16;
    const u32 l0 = lane == 0 ? 0u : BIG;
    const bool conv = w < 224;
    const rsrc_t rws = mkrs(ws, WS_END);
    const v4u ones = {0x3f803f80u, 0x3f803f80u, 0x3f803f80u, 0x3f803f80u};
    const f4 z4 = {0, 0, 0, 0};
    lch* const cnt = L + L_CNT;
    lch* const sc = L + L_SCR + wv * 1024;
    const u32 img = WS_IMG + (u32)hd * IP;
    auto hpos = [](int r, int gq, int pads) {
        const int c = r >> 2, grp = (gq & 2) + ((gq ^ c ^ (c >> 1)) & 1);
        return grp * (12 + pads) + c * 3 + (r & 3);
    };
    TS(0);
    TC(62);

    u32 tag;
    {
        const int G = w >> 5, tile = w & 31, cch = X, pp = wq;
        v4u kr, vr, qr, kh, h0, h1, wr, sr;
        v2u skw;
        u32 dd;
        {
            const u32 n = conv ? SD2 : 0u;
            const rsrc_t rk = mkrs(k, n), rv = mkrs(v, n), rh = mkrs(hk, n), rq = mkrs(q, SD2);
            const u32 ro = (u32)((lane >> 3) * 896 + (lane & 7) * 16), so = (u32)((64 * tile + 8 * wv) * 896 + G * 128);
            kr = ld16s<0>(rk, ro, so);
            vr = ld16s<0>(rv, ro, so);
            kh = ld16s<0>(rk, tid < 32 && tile ? ro : BIG, tile ? so - 3584 : 0u);
            skw = ld8s<0>(mkrs(sk, 3584), (u32)(((lane >> 4) & 1) * 32 + (lane & 3) * 8), (u32)(G * 512 + wv * 64));
            const u32 epoch = ld4<16>(rws, 0);
            qr = ld16s<0>(rq, conv ? ro : BIG, so);
            {
                const int r = lane >> 1, rg = r >> 3, j7 = (r & 7) == 7, j = j7 ? (pi >= 7 ? pi - 7 : pi) : r & 7;
                const int row = 28 * (j7 ? 14 + (pi >= 7) : pi) + (j < 4 ? 4 * rg + j : 12 + 3 * rg + j);
                const bool isw = wv < 6;
                const rsrc_t rw_ = mkrs(isw ? wqkv : wo, isw ? (pi < 14 ? 448u * 2688u : 0u) : 448u * 896u);
                wr = ld16s<0>(rw_, !isw && j7 ? BIG : (u32)(row * (isw ? 2688 : 896) + (lane & 1) * 16),
                              (u32)(hd * 56 + (wv & 1) * 32 + (isw ? (wv >> 1) * 896 : 0)));
            }
            h0 = ld16s<0>(rh, lb, (u32)(w * 8192 + wv * 1024));
            h1 = ld16<0>(rh, (tid & 255) ? (u32)(w * 8192 + wv * 1024 - 16) + lb : BIG);
            dd = ld4<0>(mkrs(sd, 896), (u32)w * 4);
            sr = ld16s<0>(rq, lane < 56 ? lb : BIG, (u32)(8 * w + wv) * 896);
            if (tid < 16) lw4(cnt + tid * 4, 0);
            if (tid >= 64 && tid < 128) lw16(L + L_OWNB + (lane >> 5) * USTR + 4096 + (lane & 31) * 16, v4u{0, 0, 0, 0});
            if (tid >= 128 && tid < 144) lw16(L + lane * HSTR, v4u{0, 0, 0, 0});
            tag = __builtin_amdgcn_readfirstlane(epoch) + 1u;
            asm volatile("" ::"s"(tag));
        }
        {
            lch* const KS = L + L_WO;
            auto swz = [](int r) { return ((r >> 1) & 1) | (((r >> 3) & 3) << 1); };
            const int r1 = 8 * wv + (lane >> 3), c1 = lane & 7;
            lw16(KS + (r1 + 4) * 128 + ((c1 ^ swz(r1 + 4)) << 4), kr);
            lw16(KS + 8704 + r1 * 128 + ((c1 ^ swz(r1)) << 4), vr);
            if (tid < 32) lw16(KS + r1 * 128 + ((c1 ^ swz(r1)) << 4), kh);
            lw16(KS + 16896 + r1 * 128 + ((c1 ^ swz(r1)) << 4), qr);
            wgbar();
            TS(24);
            const int r2 = 32 * (lane >> 5) + 8 * (lane & 3) + ((lane >> 2) & 3);
            lch* p0 = KS + r2 * 128 + ((wv ^ swz(r2)) << 4) + ((lane >> 4) & 1) * 8;
            lch* p2 = KS + (r2 + 8) * 128 + ((wv ^ swz(r2 + 8)) << 4) + ((lane >> 4) & 1) * 8;
            const v2u k0 = ltr(p0), k1 = ltr(p0 + 512), k2 = ltr(p2), v0 = ltr(p0 + 8704), v1 = ltr(p0 + 8704 + 512);
            const v2u q0 = ltr(p0 + 16896), q1 = ltr(p0 + 16896 + 512);
            lw16(sc + lb, wr);
            const float kf[11] = {bhi(k0.x), blo(k0.y), bhi(k0.y), blo(k1.x), bhi(k1.x), blo(k1.y),
                                  bhi(k1.y), blo(k2.x), bhi(k2.x), blo(k2.y), bhi(k2.y)};
            const float vf[8] = {blo(v0.x), bhi(v0.x), blo(v0.y), bhi(v0.y), blo(v1.x), bhi(v1.x), blo(v1.y), bhi(v1.y)};
            const float w0 = blo(skw.x), w1 = bhi(skw.x), w2 = blo(skw.y), w3 = bhi(skw.y);
            float r[8];
#pragma unroll
            for (int j = 0; j < 8; ++j) r[j] = fm(kf[j + 3], w0, fm(kf[j + 2], w1, fm(kf[j + 1], w2, kf[j] * w3))) * vf[j];
            const u32 to = conv ? (u32)(((lane >> 4) & 1) * 16384 + (lane >> 5) * 256 + n0 * 16) : BIG;
            const u32 ts = (u32)((8 * G + wv) * 32768 + tile * 512);
            st16s<16>(v4u{pk(r[7], r[6]), pk(r[5], r[4]), pk(r[3], r[2]), pk(r[1], r[0])}, rws, to, WS_KVT + ts);
            st16s<16>(cat(q0, q1), rws, to, WS_QT + ts);
            lch* tr = sc + (8 * gq + (n0 >> 2)) * 32 + (n0 & 3) * 8;
            v4u wf = cat(ltr(tr), ltr(tr + 128));
            const int hr = n0 + (n0 * 43 >> 7);
            u32 wo_ = lb;
            if (wv & 1) {
                wo_ = n0 < 12 ? (u32)hpos(hr, gq, wv == 3) * 16 : wv == 3 && n0 == 12 ? (u32)(gq * 13 + 12) * 16 : BIG;
                if (n0 >= 12) wf = ones;
            }
            const u32 wt = wv == 1 ? HQ : wv == 3 ? HK : wv == 5 ? HV : wv == 7 ? 1024u : wv == 6 ? 0u : (u32)wv * 512;
            st16s<16>(wf, rws, wv < 6 && pi >= 14 ? BIG : wo_, img + (wv < 6 ? pi * WSTEP : L_WO + pi * OSTEP) + wt);
            TS(27);
        }
        {
            const u32 d[8] = {h1.x, h1.y, h1.z, h1.w, h0.x, h0.y, h0.z, h0.w};
            u32 a[7];
#pragma unroll
            for (int j = 0; j < 7; ++j) a[j] = __builtin_amdgcn_alignbit(d[j + 1], d[j], 16);
            lch* p = L + cch * 8 * HSTR + 16 + pp * 1024 + lb;
#pragma unroll
            for (int s = 0; s < 4; ++s) {
                lw16(p + 2 * s * HSTR, v4u{d[4 - s], d[5 - s], d[6 - s], d[7 - s]});
                lw16(p + (2 * s + 1) * HSTR, v4u{a[3 - s], a[4 - s], a[5 - s], a[6 - s]});
            }
            float s1 = 0, s2 = 0;
#pragma unroll
            for (int j = 0; j < 4; ++j) { s1 = dot2(sr[j], 0x3f803f80u, s1); s2 = dot2(sr[j], sr[j], s2); }
            s1 = wave_sum(s1) * (1.f / 448);
            s2 = wave_sum(s2) * (1.f / 448);
            const float xr = __builtin_amdgcn_rsqf(fmaxf(s2 - s1 * s1, 0.f) + 1e-5f);
            if (lane == 0) lw8(cnt + 64 + wv * 8, v2u{(bc<u32>(xr) & ~255u) | (tag & 255u), (bc<u32>(-s1 * xr) & ~255u) | ((tag >> 8) & 255u)});
        }
        drain();
        arrive(tag, rws, conv ? l0 : BIG, WS_F1 + (u32)(8 * G + wv) * FP + tile * 4);
        TS(1);
        const u32 f1o = lane < 4 && conv ? (u32)lane * 4 : BIG, f1s = WS_F1 + (u32)(w >> 2) * FP + wv * 16;
        wgbar();
        if (wv == 7) {
            arrive(tag, rws, l0, img + 104 * 1024 + pi * 4);
            st16s<16>(lr16(cnt + 64 + (lane & 3) * 16), rws, lane < 4 ? lb : BIG, WS_ST + (u32)w * 64);
        }
        const u32 f1 = probe(rws, f1o, f1s);

        v4u wb[10];
        if (conv) {
            v4u ha[2][8], ub_[2][8];
            {
                lch* p = L + (cch * 8 + ((7 - n0) & 7)) * HSTR + ((n0 >> 3) + gq + 8 * pp) * 16;
#pragma unroll
                for (int c = 0; c < 8; ++c)
#pragma unroll
                    for (int e = 0; e < 2; ++e) ha[e][c] = lr16(p + c * 512 + e * 64);
            }
            wait_from(f1, rws, f1o, f1s, tag);
            TS(2);
            v2u qt[2];
            v4u st[2];
            {
                const u32 so = (u32)((w >> 1) * 16384 + wv * 2048 + (w & 1) * 32);
                const v4u u0 = ld16s<16>(rws, (u32)((lane >> 1) * 64 + (lane & 1) * 16), WS_KVT + so);
                u32 fw;
                if (wv == 7) fw = probe(rws, lane < 16 ? (u32)lane * 4 : BIG, img + 104 * 1024);
#pragma unroll
                for (int h = 0; h < 2; ++h) qt[h] = ld8s<16>(rws, (u32)((lane >> 1) * 64 + (lane & 1) * 8 + h * 16), WS_QT + so);
                lw16(L + L_OWNB + (lane & 1) * USTR + (255 - 32 * wv - (lane >> 1)) * 16, u0);
                if (wv == 7) wait_from(fw, rws, lane < 16 ? (u32)lane * 4 : BIG, img + 104 * 1024, tag);
            }
            lch* const ub = L + L_OWNB + cch * USTR + (2032 - 1792 + 64 * pp - 16 * n0 + 8 * gq) * 2;
            lch* const rw = L + L_HALO + wv * 1024 + (gq * 16 + (n0 ^ (gq * 4))) * 16;
            v2u u[2];
            auto ldst = [&]() {
#pragma unroll
                for (int h = 0; h < 2; ++h) st[h] = ld16s<16>(rws, 2 * lb + h * 16, WS_ST + (u32)wv * 2048);
            };
            auto finish = [&]() {
                const int col = lane >> 2, qd = lane & 3;
                lch* rr = L + L_HALO + wq * 8192 + (qd * 16 + (col ^ (qd * 4))) * 16;
                f4 p[8];
#pragma unroll
                for (int j = 0; j < 8; ++j) p[j] = bc<f4>(lr16(rr + j * 1024));
                sb();
#pragma unroll 1
                for (u32 it = 0; it < SPIN; ++it) {
                    const u32 t1 = tag >> 8;
                    const u32 bad = xor_or(st[0].x, tag, xor_or(st[0].z, tag, xor_or(st[1].x, tag, st[1].z ^ tag))) |
                                    xor_or(st[0].y, t1, xor_or(st[0].w, t1, xor_or(st[1].y, t1, st[1].w ^ t1)));
                    if (__all((bad & 255u) == 0)) break;
                    __builtin_amdgcn_s_sleep(4);
                    ldst();
                }
                f4 y[2], x[2];
#pragma unroll
                for (int h = 0; h < 2; ++h) {
                    const float d = h ? bhi(dd) : blo(dd);
                    y[h] = (p[4 * h] + p[4 * h + 1]) + (p[4 * h + 2] + p[4 * h + 3]);
                    y[h][0] = fm(bhi(u[h].y), d, y[h][0]);
                    y[h][1] = fm(blo(u[h].y), d, y[h][1]);
                    y[h][2] = fm(bhi(u[h].x), d, y[h][2]);
                    y[h][3] = fm(blo(u[h].x), d, y[h][3]);
                    const f4 qv = {blo(qt[h].x), bhi(qt[h].x), blo(qt[h].y), bhi(qt[h].y)};
#pragma unroll
                    for (int e = 0; e < 4; ++e)
                        x[h][e] = fm(fm(qv[e], bc<float>(st[e >> 1][2 * (e & 1)]), bc<float>(st[e >> 1][2 * (e & 1) + 1])), y[h][e], qv[e]);
                }
                const u32 o = (u32)(col * 14336 + qd * 16), so = (u32)(16 * wv * 14336 + w * 64);
                st16s<16>(v4u{pk(x[0][0], x[0][1]), pk(x[0][2], x[0][3]), pk(x[1][0], x[1][1]), pk(x[1][2], x[1][3])}, rws, o, WS_X1 + so);
                st16s<16>(v4u{pk(y[0][0], y[0][1]), pk(y[0][2], y[0][3]), pk(y[1][0], y[1][1]), pk(y[1][2], y[1][3])}, rws, o, WS_YT + so);
            };
            f4 acc[4];
            auto macs = [&](int part, int s0, int s1) {
#pragma unroll
                for (int s = s0; s < s1; ++s)
#pragma unroll
                    for (int e = 0; e < 2; ++e)
#pragma unroll
                        for (int t = 0; t < 4; ++t) {
                            const int c = 4 * part + t - s;
                            if (c >= 0) acc[t] = mma(ha[e][c], ub_[e][s], s + e ? acc[t] : z4);
                        }
            };
            wgbar();
            TS(3);
#pragma unroll
            for (int s = 0; s < 4; ++s)
#pragma unroll
                for (int e = 0; e < 2; ++e) ub_[e][s] = lr16(ub + (7 - s) * 512 + e * 64);
#pragma unroll
            for (int h = 0; h < 2; ++h) u[h] = lr8(L + L_OWNB + h * USTR + (2044 - 256 * wv - 4 * lane) * 2);
            ldst();
            sb();
            macs(0, 0, 4);
#pragma unroll
            for (int t = 0; t < 4; ++t) lw16(rw + t * 8192, bc<v4u>(acc[t]));
            if (X) {
#pragma unroll
                for (int j = 0; j < 10; ++j) wb[j] = ldk<16>(rws, lb, img + wq * 10240, j);
            }
            wgbar();
            if (X == 0) finish();
            sb();
            macs(1, 0, 3);
#pragma unroll
            for (int t = 0; t < 4; ++t) pin(acc[t]);
#pragma unroll
            for (int s = 4; s < 8; ++s)
#pragma unroll
                for (int e = 0; e < 2; ++e) ub_[e][s] = lr16(ub + (7 - s) * 512 + e * 64);
            if (X == 0) {
                drain();
                arrive(tag, rws, lane < 4 ? (u32)lane * 4 * FP : BIG, WS_F2 + (u32)(wv * 16 + w / 56) * FP + (w % 56) * 4);
                TS(4);
            } else {
#pragma unroll
                for (int j = 0; j < 10; ++j) lw16(L + wq * 10240 + j * 1024 + lb, wb[j]);
            }
#pragma unroll
            for (int j = 0; j < 8; ++j) wb[j] = ldk<16>(rws, lb, img + 40960 + wv * 8192, j);
            sb();
            macs(1, 3, 8);
#pragma unroll
            for (int t = 0; t < 4; ++t) pin(acc[t]);
            wgbar();
#pragma unroll
            for (int t = 0; t < 4; ++t) lw16(rw + t * 8192, bc<v4u>(acc[t]));
#pragma unroll
            for (int j = 0; j < 8; ++j) lw16(L + 40960 + wv * 8192 + j * 1024 + lb, wb[j]);
            wgbar();
            if (X) finish();
        } else {
            if (wv == 7) wait_flag(rws, lane < 16 ? (u32)lane * 4 : BIG, img + 104 * 1024, tag);
            wgbar();
#pragma unroll 1
            for (int h = 0; h < 13; ++h) lw16(L + (wv * 13 + h) * 1024 + lb, ld16s<16>(rws, lb, img + (u32)(wv * 13 + h) * 1024));
            wgbar();
        }
    }
    lch* const wl = L + lb;
    lch* const wh = L + hpos((n0 & 3) == 3 ? n0 - 1 : n0, gq, 0) * 16;
    lch* const whk = L + HK + ((n0 & 3) == 3 ? hpos(n0 & 12, gq, 1) - (n0 >> 2) * 3 + 12 : hpos(n0, gq, 1)) * 16;
    if (X) {
        sig_lds(cnt + 20, lane);
        if (wq < 3) {
            f4 c0 = z4, c1 = z4;
            lch* ph = wq == 1 ? whk : wh + (wq == 0 ? HQ : HV);
#pragma unroll
            for (int s = 0; s < 14; ++s) {
                c0 = mma(lr16(wl + wq * 1024 + s * WSTEP), ones, c0);
                c1 = mma(lr16(ph + s * WSTEP), ones, c1);
            }
            if (n0 == 0) {
                lw16(L + L_CS + wq * 128 + gq * 16, bc<v4u>(c0));
                lw16(L + L_CS + wq * 128 + 64 + gq * 16, bc<v4u>(c1));
            }
            sig_lds(cnt + 28, lane);
        }
        if (conv) {
            drain();
            arrive(tag, rws, lane < 4 ? (u32)lane * 4 * FP : BIG, WS_F2 + (u32)(wv * 16 + w / 56) * FP + (w % 56) * 4);
            TS(4);
        }
    }
    TS(5);

#define IDS()                                                                                              \
    int ln_ = lane0, wv_ = wv0, w_ = w0;                                                                   \
    asm volatile("" : "+v"(ln_)); spin(wv_); spin(w_);                                                     \
    const int lane = ln_, wv = wv_, w = w_, hd = (w >> 3) & 15, X = wv >> 2, wq = wv & 3;                  \
    const int tau = 2 * (w & 7) + (w >> 7) + 16 * X, b = 4 * tau + wq, n0 = lane & 15, gq = lane >> 4;     \
    const u32 lb = lane * 16;                                                                              \
    (void)hd, (void)b, (void)n0, (void)gq, (void)lb
    const int lane0 = lane, wv0 = wv, w0 = w;
    const u32 gt = (0x2000u | (tag & 0x1fffu) | (tag & 0x2000u) << 2) << 16;
    u32 kvo = WS_KV + (u32)(hd * 32 + tau) * KP + wq * 2048, fo, hs, f0, f1, f2;
    v4u qf;
    f4 c0, c1;
    v4u yo;
    auto kvproj = [&](auto first_, u32 lo, u32 so) {
        constexpr bool FIRST = decltype(first_)::value;
        constexpr int ly = FIRST ? 0 : 1, PC = FIRST ? 896 : 1024;
        IDS();
        lch* const sc = L + L_SCR + wv * 1024;
        lch* const t0 = sc + gq * 128 + (n0 >> 3) * 64 + (n0 & 3) * 16 + ((n0 >> 2) & 1) * 8;
        const int ch = 16 + 3 * gq + (n0 >> 2);
        lch* const t1 = sc + (ch >> 1) * 64 + (n0 & 3) * 16 + (ch & 1) * 8;
        v4u xf[16], wf[2][4];
        auto ytr = [&](v4u& x) {
            lw16(sc + lb, x);
            x = cat(ltr(t0), ltr(t1));
        };
        auto ldw = [&](int s, int j) {
            wf[s & 1][j] = lr16((j == 0 ? whk : j == 3 ? wh + HV : wl + j * 1024) + s * WSTEP);
        };
        const bool real = tau - 4 + wq >= 0;
        fo = lane < 4 && real ? (u32)lane * 4 : BIG;
        u32 as = kvo + 8192 - (u32)wq * 2044, vo = n0 * 64 + gq * 16 + 1024;
        hs = real ? kvo - (u32)(4 - wq) * KP - (u32)wq * 2048 : 0u;
        spin(as); spin(hs);
        f4 ak0, ak1, av0, av1, ag;
#pragma unroll 1
        for (u32 it = 0; it < SPIN; ++it) {
            pin(lo);
#pragma unroll
            for (int j = 0; j < 16; ++j) {
                const int s = (j + 14) & 15;
                xf[s] = ld16s<16>(rws, lo + (s & 3) * PC, so + (s >> 2) * 4 * PC);
                sb();
            }
            if (FIRST && w == 0 && wv == 0) st4<16>(tag, rws, lane ? BIG : 0u);
#pragma unroll
            for (int j = 0; j < 4; ++j) ldw(0, j);
            u32 bad = 0;
            if (FIRST) {
                ytr(xf[14]);
                ytr(xf[15]);
                ytr(xf[0]);
                ytr(xf[1]);
            } else {
                bad = xor_or(xf[14].w, gt, xf[15].w ^ gt);
            }
            sb();
#pragma unroll
            for (int s = 0; s < 14; ++s) {
                if (!FIRST) bad = xor_or(xf[s].w, gt, bad);
                xf[s].w = __builtin_amdgcn_perm(xf[14 + s / 7][(s % 7) >> 1], xf[s].w, (s % 7) & 1 ? 0x07060100u : 0x05040100u);
                if (FIRST && s < 12) ytr(xf[s + 2]);
                if (s == (FIRST ? 11 : 13)) {
                    arrive(tag, rws, lane || bad > 0xffffu ? BIG : 0u, as);
                    probe_(f0, rws, fo, hs + 8192);
                }
                sb();
                auto slot = [&](int j) {
                    if (s < 13) ldw(s + 1, j);
                    sb();
                };
                ag = mma(xf[s], xf[s], s ? ag : z4);
                slot(0);
                ak1 = mma(wf[s & 1][0], xf[s], s ? ak1 : z4);
                slot(1);
                ak0 = mma(wf[s & 1][1], xf[s], s ? ak0 : z4);
                slot(2);
                av0 = mma(wf[s & 1][2], xf[s], s ? av0 : z4);
                slot(3);
                av1 = mma(wf[s & 1][3], xf[s], s ? av1 : z4);
                sb();
            }
            if (FIRST || __all(bad <= 0xffffu)) break;
            TN(48, it + 1);
            drain();
            __builtin_amdgcn_s_sleep(4);
        }
        TS(8 + 8 * ly);
        if (FIRST) wait_lds(cnt + 28, 3);
        float nm, r, tk[7], tv[7];
        {
            const f4 ck0 = bc<f4>(lr16(L + L_CS + 128 + gq * 16)), ck1 = bc<f4>(lr16(L + L_CS + 192 + gq * 16));
            const f4 cv0 = bc<f4>(lr16(L + L_CS + 256 + gq * 16)), cv1 = bc<f4>(lr16(L + L_CS + 320 + gq * 16));
            nm = ak1[3] * (-1.f / 448);
            const float d = (n0 & 2) ? ((n0 & 1) ? ag[3] : ag[2]) : ((n0 & 1) ? ag[1] : ag[0]);
            r = __builtin_amdgcn_rsqf(fmaxf(fm(-nm, nm, rows_sum(gq == (n0 >> 2) ? d : 0.f) * (1.f / 448)), 0.f) + 1e-5f);
            probe_(f1, rws, fo, hs + 8192);
#pragma unroll
            for (int e = 0; e < 7; ++e) {
                tk[e] = (e < 4 ? fm(nm, ck0[e], ak0[e]) : fm(nm, ck1[e - 4], ak1[e - 4])) * r;
                tv[e] = (e < 4 ? fm(nm, cv0[e], av0[e]) : fm(nm, cv1[e - 4], av1[e - 4])) * r;
            }
        }
        v4u kf = {pk(tk[0], tk[1]), pk(tk[2], tk[3]), pk(tk[4], tk[5]), pk(tk[6], bc<float>(gt))};
        st16s<16>(kf, rws, lb, kvo);
        v4u vf = {pk(tv[0], tv[1]), pk(tv[2], tv[3]), pk(tv[4], tv[5]), pk(tv[6], bc<float>(gt))};
        st16s<16>(vf, rws, vo, kvo);
        sb();
        TS(36 + 8 * ly);
        {
            v4u qw[8];
            auto ldq = [&](int j) { qw[j & 7] = lr16(((j & 1) ? wh + HQ : wl) + (j >> 1) * WSTEP); };
#pragma unroll
            for (int j = 0; j < 8; ++j) ldq(j);
            if (ly) wait_lds(cnt + 16 + X * 4, 4 + 4 * X);
            lch* ob = L + (X ? L_OWNB + (3 - wq) * 2048 : L_OWNA + wq * 2048);
            kf.w &= 0xffffu;
            lw16(ob + lb, kf);
            lw8(ob + 1024 + n0 * 32 + gq * 8, v2u{vf.x, vf.y});
            lw8(ob + 1536 + n0 * 32 + gq * 8, v2u{vf.z, (vf.w & 0xffffu) | 0x3f800000u});
            const f4 cq0 = bc<f4>(lr16(L + L_CS + gq * 16)), cq1 = bc<f4>(lr16(L + L_CS + 64 + gq * 16));
            sb();
            f4 aq[2];
#pragma unroll
            for (int j = 0; j < 28; ++j) {
                aq[j & 1] = mma(qw[j & 7], xf[j >> 1], j > 1 ? aq[j & 1] : z4);
                if (j + 8 < 28) ldq(j + 8);
                if (j == 4) probe_(f2, rws, fo, hs + 8192);
                sb();
            }
            TS(9 + 8 * ly);
            const float rq = r * (0.18898223650461362f * 1.4426950408889634f);
            float t[7];
#pragma unroll
            for (int e = 0; e < 7; ++e) t[e] = (e < 4 ? fm(nm, cq0[e], aq[0][e]) : fm(nm, cq1[e - 4], aq[1][e - 4])) * rq;
            qf = v4u{pk(t[0], t[1]), pk(t[2], t[3]), pk(t[4], t[5]), pk(t[6], 1.f)};
            pin(qf);
        }
    };

    wait_flag(rws, lane < 56 ? (u32)lane * 4 : BIG, WS_F2 + (u32)b * FP, tag);
    sig_lds(cnt + X * 4, lane);
    wait_lds(cnt + X * 4, 4);
    TS(6);
    spin(kvo);
    kvproj(std::true_type{}, lane < 56 ? lb : BIG, WS_X1 + (u32)b * 14336);
#pragma unroll 1
    for (int ly = 0;; ++ly) {
        u32 oo, po, ao_;
        {
            IDS();
            const bool real = tau - 4 + wq >= 0;
            lch* ak = L + (X ? L_OWNB + (3 - wq) * 2048 : L_HALO + wq * 2048) + lb;
            lch* av = ak + 1024 - lane * 8;
            lch* hp = L + L_HALO + (X ? 3 - wq : wq) * 8192 + lb;
            lch* hv = hp + 1024 - lane * 8;
            const int dl = n0 - 4 * gq;
            f4 mf, ml;
#pragma unroll
            for (int e = 0; e < 4; ++e) {
                mf[e] = (e > dl) != (bool)X ? 0.f : -1e30f;
                ml[e] = (e > dl) == (bool)X ? 0.f : -1e30f;
            }
            u32 kpad = real ? 0u : 0xf1490000u, hl = real ? lb : BIG, hx = X ? 6144 : 0, gh = real ? gt : 0u;
            oo = WS_O + (u32)(ly * 128 + b) * XP;
            po = lane < 16 && lane != hd ? (u32)(hd & 3) * 256 + lane * 4 : BIG;
            ao_ = lane < 4 ? (u32)lane * 256 + hd * 4 : BIG;
            u32 os = oo + hd * 1024, lb2 = lb;
            lch* c_kv = cnt + 8 + X * 4;
            asm volatile("" : "+v"(mf), "+v"(ml), "+v"(ak), "+v"(av), "+v"(hp), "+v"(hv), "+v"(po), "+v"(ao_), "+v"(c_kv), "+v"(lb2));
            spin(os); spin(oo); spin(kpad); spin(hx); spin(gh);
            wait3<2>(f0, f1, f2, rws, fo, hs + 8192, tag);
            TS(10 + 8 * ly);
            v4u hb[8];
            v4u xo;
#pragma unroll 1
            for (u32 it = 0; it < SPIN; ++it) {
                pin(hl);
#pragma unroll
                for (int j = 0; j < 8; ++j) hb[j] = ld16s<16>(rws, hl + (j & 1) * 1024, hs + ((j >> 1) * 2048 ^ hx));
                u32 bad = 0;
#pragma unroll
                for (int j = 0; j < 8; ++j) bad = xor_or(hb[j].w, gh, bad);
                if (__all(bad <= 0xffffu)) break;
                TN(49 + ly, it + 1);
                __builtin_amdgcn_s_sleep(4);
            }
            keep(f0, f1, f2);
            TS(38 + 8 * ly);
            if (ly == 0) {
                xo = ld16s<16>(rws, lane < 56 ? lb : BIG, WS_X1 + (u32)b * 14336 + hd * 896);
                yo = ld16s<16>(rws, lane < 56 ? lb : BIG, WS_YT + (u32)b * 14336 + hd * 896);
            }
            wait_lds(cnt + 16 + (X ^ 1) * 4, 4 * (ly + 1));
#pragma unroll
            for (int j = 0; j < 4; ++j) {
                hb[2 * j].w = (hb[2 * j].w & 0xffffu) | kpad;
                lw16(hp + j * 2048, hb[2 * j]);
                lw8(hv + j * 2048, v2u{hb[2 * j + 1].x, hb[2 * j + 1].y});
                lw8(hv + j * 2048 + 512, v2u{hb[2 * j + 1].z, (hb[2 * j + 1].w & 0xffffu) | 0x3f800000u});
            }
            sig_lds(c_kv, lane);
            wait_lds(c_kv, 4 * (ly + 1));
            TS(11 + 8 * ly);
            v4u kf[17];
#pragma unroll
            for (int u = 0; u < 17; ++u) kf[u] = lr16(ak + u * 2048);
            sb();
            f4 s[17];
            v2u vf[17][2];
            float mx;
#pragma unroll
            for (int u = 0; u < 17; ++u) {
                s[u] = mma(kf[u], qf, u == 0 ? mf : u == 16 ? ml : z4);
                vf[u][0] = ltr(av + u * 2048);
                vf[u][1] = ltr(av + u * 2048 + 512);
                if (u == 2) mx = fmaxf(fmaxf(s[0][0], s[0][1]), fmaxf(s[0][2], s[0][3]));
                if (u > 2) mx = fmaxf(fmaxf(fmaxf(fmaxf(mx, s[u - 2][0]), s[u - 2][1]), s[u - 2][2]), s[u - 2][3]);
                sb();
            }
#pragma unroll
            for (int u = 15; u < 17; ++u) mx = fmaxf(fmaxf(fmaxf(fmaxf(mx, s[u][0]), s[u][1]), s[u][2]), s[u][3]);
            sig_lds(c_kv + 8, lane);
            mx = rows_max(mx);
            auto pe = [&](int u) {
                return v2u{pk(__builtin_amdgcn_exp2f(s[u][0] - mx), __builtin_amdgcn_exp2f(s[u][1] - mx)),
                           pk(__builtin_amdgcn_exp2f(s[u][2] - mx), __builtin_amdgcn_exp2f(s[u][3] - mx))};
            };
            f4 o0, o1;
            {
                const v4u p = cat(pe(16), v2u{0, 0});
                o0 = mma(cat(vf[16][0], vf[16][0]), p, z4);
                o1 = mma(cat(vf[16][1], vf[16][1]), p, z4);
            }
            if (ly == 0) {
                lch* const sc = L + L_SCR + wv * 1024;
                lch* const t0 = sc + gq * 128 + (n0 >> 3) * 64 + (n0 & 3) * 16 + ((n0 >> 2) & 1) * 8;
                const int ch = 16 + 3 * gq + (n0 >> 2);
                lch* const t1 = sc + (ch >> 1) * 64 + (n0 & 3) * 16 + (ch & 1) * 8;
                lw16(sc + lb, xo);
                xo = cat(ltr(t0), ltr(t1));
                lw16(sc + lb, yo);
                yo = cat(ltr(t0), ltr(t1));
                c0 = f4{blo(xo.x), bhi(xo.x), blo(xo.y), bhi(xo.y)};
                c1 = f4{blo(xo.z), bhi(xo.z), blo(xo.w), 0.f};
            }
            sb();
#pragma unroll
            for (int u = 0; u < 8; ++u) {
                const v4u pf = cat(pe(2 * u), pe(2 * u + 1));
                o0 = mma(cat(vf[2 * u][0], vf[2 * u + 1][0]), pf, o0);
                o1 = mma(cat(vf[2 * u][1], vf[2 * u + 1][1]), pf, o1);
                if (u == 4) {
                    arrive(tag, rws, ao_, oo + 16384);
                    probe_(f0, rws, po, oo + 16384);
                }
                if (u == 6) probe_(f1, rws, po, oo + 16384);
                sb();
            }
            const float inv = __builtin_amdgcn_rcpf(o1[3]);
            o0 *= inv;
            o1 *= inv;
            st16s<16>(v4u{pk(o0[0], o0[1]), pk(o0[2], o0[3]), pk(o1[0], o1[1]), pk(o1[2], bc<float>(gt))}, rws, lb2, os);
            probe_(f2, rws, po, oo + 16384);
            sb();
            TS(12 + 8 * ly);
        }
        u32 ss;
        {
            IDS();
            lch* wp = L + L_WO + lb;
            const int hr = n0 < 12 ? n0 + (n0 * 43 >> 7) : 0;
            lch* wph = L + L_WO + 1024 + hpos(ly ? hr : (n0 & 3) == 3 ? n0 - 1 : n0, gq, 0) * 16;
            v4u wg[32];
            auto ldg = [&](int j) { wg[j] = lr16(((j & 1) ? wph : wp) + (j >> 1) * OSTEP); };
#pragma unroll
            for (int j = 0; j < 32; ++j) ldg(j);
            u32 lb2 = lb;
            ss = WS_S1 + (u32)b * SP;
            spin(ss);
            wait3<1>(f0, f1, f2, rws, po, oo + 16384, tag);
            TS(14 + 8 * ly);
            f4 a0, a1;
#pragma unroll 1
            for (u32 it = 0; it < SPIN; ++it) {
                pin(lb2);
                v4u of[16];
#pragma unroll
                for (int s = 0; s < 16; ++s) of[s] = ldk<16>(rws, lb2, oo, s);
                sb();
                u32 bad = 0;
#pragma unroll
                for (int s = 0; s < 16; ++s) {
                    bad = xor_or(of[s].w, gt, bad);
                    if (s == 12) arrive(tag, rws, ly ? BIG : ao_, ss + 4096);
                    a0 = mma(wg[2 * s], of[s], s ? a0 : c0);
                    a1 = mma(wg[2 * s + 1], of[s], s ? a1 : c1);
                    sb();
                }
                if (__all(bad <= 0xffffu)) break;
                TN(51 + ly, it + 1);
                __builtin_amdgcn_s_sleep(4);
            }
            keep(f0, f1, f2);
            c0 = a0;
            c1 = a1;
            TS(33 + 8 * ly);
        }
        if (ly) break;
        {
            IDS();
            probe_(f0, rws, po, ss + 4096);
            const float s1 = rows_sum(((c0[0] + c0[1]) + (c0[2] + c0[3])) + ((c1[0] + c1[1]) + c1[2]));
            const float s2 = rows_sum(fm(c0[0], c0[0], fm(c0[1], c0[1], fm(c0[2], c0[2], c0[3] * c0[3]))) + fm(c1[0], c1[0], fm(c1[1], c1[1], c1[2] * c1[2])));
            probe_(f1, rws, po, ss + 4096);
            st16s<16>(v4u{bc<u32>(s1), bc<u32>(s2), tag, tag}, rws, lane < 16 ? lb : BIG, ss + hd * 256);
            TS(34);
            u32 xs = WS_X3 + (u32)b * XP, lb2 = lb;
            spin(xs);
            kvo += 512 * KP;
            const float y[7] = {blo(yo.x), bhi(yo.x), blo(yo.y), bhi(yo.y), blo(yo.z), bhi(yo.z), blo(yo.w)};
            __builtin_amdgcn_s_sleep(2);
            probe_(f2, rws, po, ss + 4096);
            wait3<1>(f0, f1, f2, rws, po, ss + 4096, tag);
            TS(15);
            v4u sp[4];
#pragma unroll 1
            for (u32 it = 0; it < SPIN; ++it) {
                pin(lb2);
#pragma unroll
                for (int m = 0; m < 4; ++m) sp[m] = ld16s<16>(rws, lb2 + m * 1024, ss);
                if (it == 0) {
                    __builtin_amdgcn_s_sleep(2);
                    arrive(tag, rws, ao_, xs + 16384);
                }
                if (__all(xor_or(sp[0].z, tag, xor_or(sp[1].z, tag, xor_or(sp[2].z, tag, sp[3].z ^ tag))) == 0)) break;
                TN(53, it + 1);
                __builtin_amdgcn_s_sleep(4);
            }
            keep(f0, f1, f2);
            probe_(f0, rws, po, xs + 16384);
            const float m1 = rows_sum((bc<float>(sp[0].x) + bc<float>(sp[1].x)) + (bc<float>(sp[2].x) + bc<float>(sp[3].x))) * (1.f / 448);
            const float m2 = rows_sum((bc<float>(sp[0].y) + bc<float>(sp[1].y)) + (bc<float>(sp[2].y) + bc<float>(sp[3].y))) * (1.f / 448);
            const float xr = __builtin_amdgcn_rsqf(fmaxf(m2 - m1 * m1, 0.f) + 1e-5f), xc = -m1 * xr;
#pragma unroll
            for (int e = 0; e < 4; ++e) c0[e] = fm(fm(c0[e], xr, xc), y[e], c0[e]);
#pragma unroll
            for (int e = 0; e < 3; ++e) c1[e] = fm(fm(c1[e], xr, xc), y[4 + e], c1[e]);
            probe_(f1, rws, po, xs + 16384);
            st16s<16>(v4u{pk(c0[0], c0[1]), pk(c0[2], c0[3]), pk(c1[0], c1[1]), pk(c1[2], bc<float>(gt))}, rws, lb2, xs + hd * 1024);
            TS(35);
            lch* const sc = L + L_SCR + wv * 1024 + n0 * 64;
#pragma unroll
            for (int e = 0; e < 3; ++e) lw4(sc + gq * 12 + e * 4, bc<u32>(c1[e]));
            c1 = bc<f4>(lr16(sc + (gq < 3 ? gq : 0) * 16));
            pin(c1);
            __builtin_amdgcn_s_sleep(2);
            probe_(f2, rws, po, xs + 16384);
            wait3<1>(f0, f1, f2, rws, po, xs + 16384, tag);
            TS(7);
            const u32 g0 = f0, g1 = f1, g2 = f2;
            kvproj(std::false_type{}, lb, xs);
            keep(g0, g1, g2);
        }
    }
    {
        IDS();
        const rsrc_t ro = mkrs(out, SD2);
        const u32 uo = n0 * 896 + gq * 8, us = b * 14336 + hd * 56;
        st8s<0>(v2u{pk(c0[0], c0[1]), pk(c0[2], c0[3])}, ro, uo, us);
        st8s<0>(v2u{pk(c1[0], c1[1]), pk(c1[2], c1[3])}, ro, gq < 3 ? uo + 32 : BIG, us);
        TS(23);
        TC(63);
    }
}
}

extern "C" void kernel_launch(void* const* d_in, const int* in_sizes, int n_in, void* d_out, int out_size,
                              void* d_ws, size_t ws_size, hipStream_t stream) {
    (void)in_sizes; (void)n_in; (void)out_size; (void)ws_size;
    (void)hipFuncSetAttribute((const void*)k_main, hipFuncAttributeMaxDynamicSharedMemorySize, (int)L_END);
    hipLaunchKernelGGL(k_main, dim3(256), dim3(512), L_END, stream, (const u16*)d_in[0], (const u16*)d_in[1],
                       (const u16*)d_in[2], (const u16*)d_in[3], (const u16*)d_in[4], (const u16*)d_in[5],
                       (const u16*)d_in[7], (const u16*)d_in[8], (u16*)d_out, (char*)d_ws);
}
```

```cpp
#include <hip/hip_runtime.h>
#include <stdint.h>
#include <type_traits>

extern "C" __device__ int buf_swap_(int, __amdgpu_buffer_rsrc_t, int, int, int) __asm("llvm.amdgcn.raw.ptr.buffer.atomic.swap.i32");

namespace {
typedef unsigned short u16;
typedef unsigned u32;
typedef u32 v4u __attribute__((ext_vector_type(4)));
typedef u32 v2u __attribute__((ext_vector_type(2)));
typedef float f4 __attribute__((ext_vector_type(4)));
typedef float f2 __attribute__((ext_vector_type(2)));
typedef short s4 __attribute__((ext_vector_type(4)));
typedef __bf16 bf8 __attribute__((ext_vector_type(8)));
typedef __bf16 bf2 __attribute__((ext_vector_type(2)));
#define LAS __attribute__((address_space(3)))
typedef LAS char lch;
typedef __amdgpu_buffer_rsrc_t rsrc_t;
#define DEV __device__ __forceinline__

constexpr u32 SD2 = 2048u * 448u * 2u;
constexpr u32 WSTEP = 5440, OSTEP = 1792;
constexpr u32 HQ = 3072, HV = 3840, HK = 4608;
constexpr u32 L_WO = 14 * WSTEP, L_SCR = L_WO + 16 * OSTEP, L_OWNB = L_SCR + 8192, L_HALO = L_OWNB + 8192,
              L_OWNA = L_HALO + 32768, L_CS = L_OWNA + 8192, L_CNT = L_CS + 384, L_END = L_CNT + 128;
constexpr u32 HSTR = 4160, USTR = 40960 + 64;
static_assert(L_END <= 163840 && 16 * HSTR <= L_WO && 104 * 1024 <= L_OWNB, "lds map");
constexpr u32 FP = 4352;
constexpr u32 XP = 17664, SP = 5376, KP = 8448, IP = 104 * 1024 + 256;
constexpr u32 WS_F1 = FP, WS_F2 = WS_F1 + 56 * FP, WS_ST = 0xC8000, WS_KVT = 0xD0000, WS_QT = WS_KVT + SD2, WS_X1 = WS_QT + SD2,
              WS_YT = WS_X1 + SD2, WS_X3 = WS_YT + SD2, WS_O = WS_X3 + 128 * XP, WS_S1 = WS_O + 256 * XP,
              WS_KV = WS_S1 + 128 * SP, WS_IMG = WS_KV + 1024 * KP, WS_END = WS_IMG + 16 * IP;
static_assert(WS_F2 + 128 * FP <= WS_ST, "ws map");
constexpr u32 WS_TS = WS_END;
constexpr u32 BIG = 0x7ff00000u;
constexpr u32 SPIN = 1u << 15;

template <class T, class F> DEV T bc(F f) { return __builtin_bit_cast(T, f); }
DEV rsrc_t mkrs(const void* p, u32 bytes) { return __builtin_amdgcn_make_buffer_rsrc((void*)p, 0, bytes, 0x00020000); }
template <int AUX> DEV v4u ld16(rsrc_t r, u32 o) { return __builtin_amdgcn_raw_buffer_load_b128(r, o, 0, AUX); }
template <int AUX> DEV u32 ld4(rsrc_t r, u32 o) { return __builtin_amdgcn_raw_buffer_load_b32(r, o, 0, AUX); }
template <int AUX> DEV void st4(u32 v, rsrc_t r, u32 o) { __builtin_amdgcn_raw_buffer_store_b32(v, r, o, 0, AUX); }
template <int AUX> DEV v4u ld16s(rsrc_t r, u32 o, u32 so) { return __builtin_amdgcn_raw_buffer_load_b128(r, o, so, AUX); }
template <int AUX> DEV v2u ld8s(rsrc_t r, u32 o, u32 so) { return __builtin_amdgcn_raw_buffer_load_b64(r, o, so, AUX); }
template <int AUX> DEV void st16s(v4u v, rsrc_t r, u32 o, u32 so) {
    __builtin_amdgcn_raw_buffer_store_b128(v, r, o, so, AUX);
    asm volatile("s_nop 1" ::"v"(v) : "memory");
}
template <int AUX> DEV void st8s(v2u v, rsrc_t r, u32 o, u32 so) { __builtin_amdgcn_raw_buffer_store_b64(v, r, o, so, AUX); }
template <int AUX> DEV v4u ldk(rsrc_t r, u32 lb, u32 so, int j) { return ld16s<AUX>(r, lb + (j & 3) * 1024, so + (j >> 2) * 4096); }
DEV void sb() { __builtin_amdgcn_sched_barrier(0); }
DEV f4 mma(v4u a, v4u b, f4 c) { return __builtin_amdgcn_mfma_f32_16x16x32_bf16(bc<bf8>(a), bc<bf8>(b), c, 0, 0, 0); }
DEV u32 pk(float lo, float hi) { f2 f = {lo, hi}; return bc<u32>(__builtin_convertvector(f, bf2)); }
DEV float blo(u32 d) { return bc<float>(d << 16); }
DEV float bhi(u32 d) { return bc<float>(d & 0xffff0000u); }
DEV float dot2(u32 a, u32 b, float c) { return __builtin_amdgcn_fdot2_f32_bf16(bc<bf2>(a), bc<bf2>(b), c, false); }
DEV u32 xor_or(u32 a, u32 b, u32 c) { return __builtin_amdgcn_bitop3_b32(a, b, c, 0xbe); }
template <class T> DEV void spin(T& v) { v = __builtin_amdgcn_readfirstlane(v); asm volatile("" : "+s"(v)); }
template <class T> DEV void pin(T& v) { asm volatile("" : "+v"(v)); }
DEV float fm(float a, float b, float c) { return __builtin_fmaf(a, b, c); }
DEV v4u lr16(lch* p) { return *(LAS v4u*)p; }
DEV v2u lr8(lch* p) { return *(LAS v2u*)p; }
DEV void lw16(lch* p, v4u v) { *(LAS v4u*)p = v; }
DEV void lw8(lch* p, v2u v) { *(LAS v2u*)p = v; }
DEV void lw4(lch* p, u32 v) { *(LAS u32*)p = v; }
DEV v2u ltr(lch* p) { return bc<v2u>(__builtin_amdgcn_ds_read_tr16_b64_v4i16((LAS s4*)p)); }
DEV v4u cat(v2u a, v2u b) { return v4u{a.x, a.y, b.x, b.y}; }
DEV void cbar() { asm volatile("" ::: "memory"); }
DEV void wgbar() { asm volatile("s_waitcnt lgkmcnt(0)\n\ts_barrier" ::: "memory"); }
DEV void drain() { asm volatile("s_waitcnt vmcnt(0)" ::: "memory"); }
DEV float rows_max(float v) {
    auto a = __builtin_amdgcn_permlane32_swap(bc<u32>(v), bc<u32>(v), false, false);
    v = fmaxf(bc<float>(a[0]), bc<float>(a[1]));
    auto b = __builtin_amdgcn_permlane16_swap(bc<u32>(v), bc<u32>(v), false, false);
    return fmaxf(bc<float>(b[0]), bc<float>(b[1]));
}
DEV float rows_sum(float v) {
    auto a = __builtin_amdgcn_permlane32_swap(bc<u32>(v), bc<u32>(v), false, false);
    v = bc<float>(a[0]) + bc<float>(a[1]);
    auto b = __builtin_amdgcn_permlane16_swap(bc<u32>(v), bc<u32>(v), false, false);
    return bc<float>(b[0]) + bc<float>(b[1]);
}
template <int R> DEV float ror_add(float v) { return v + bc<float>(__builtin_amdgcn_update_dpp(0u, bc<u32>(v), 0x120 + R, 0xf, 0xf, false)); }
DEV float wave_sum(float v) { return rows_sum(ror_add<1>(ror_add<2>(ror_add<4>(ror_add<8>(v))))); }
DEV void arrive(u32 tag, rsrc_t r, u32 o, u32 so) { (void)buf_swap_((int)tag, r, (int)o, (int)so, 16); }
DEV u32 probe(rsrc_t r, u32 off, u32 so) { return (u32)__builtin_amdgcn_raw_ptr_buffer_atomic_add_i32(0, r, off, so, 16); }
DEV void wait_from(u32 f, rsrc_t r, u32 off, u32 so, u32 tag) {
#pragma unroll 1
    for (u32 it = 0; it < SPIN; ++it) {
        off = f == tag ? BIG : off;
        if (__all(off == BIG)) break;
        __builtin_amdgcn_s_sleep(1);
        f = probe(r, off, so);
    }
    cbar();
}
DEV void wait_flag(rsrc_t r, u32 off, u32 so, u32 tag) { wait_from(probe(r, off, so), r, off, so, tag); }
template <int N> DEV void wait3(u32& f0, u32& f1, u32& f2, rsrc_t r, u32 off, u32 so, u32 tag) {
    const unsigned long long msk = __ballot(off != BIG);
    u32 cnt = SPIN;
#define CHK(F) "v_cmp_ne_u32 vcc, %[tag], %[" F "]\n\ts_and_b64 vcc, vcc, %[msk]\n\ts_cbranch_scc0 2f\n\tv_mov_b32 %[" F "], 0\n\t" \
               "buffer_atomic_add %[" F "], %[off], %[rs], %[so] offen sc0 sc1\n\t"
    asm volatile("s_waitcnt vmcnt(%[n])\n\t" CHK("f0") "s_waitcnt vmcnt(%[n])\n\t" CHK("f1")
                 "1:\n\ts_waitcnt vmcnt(2)\n\t" CHK("f2") "s_waitcnt vmcnt(2)\n\t" CHK("f0") "s_waitcnt vmcnt(2)\n\t" CHK("f1")
                 "s_sub_u32 %[cnt], %[cnt], 1\n\ts_cbranch_scc0 1b\n2:"
                 : [f0] "+v"(f0), [f1] "+v"(f1), [f2] "+v"(f2), [cnt] "+s"(cnt)
                 : [tag] "s"(tag), [msk] "s"(msk), [off] "v"(off), [rs] "s"(r), [so] "s"(so), [n] "n"(N + 2)
                 : "memory", "vcc", "scc");
#undef CHK
}
DEV void probe_(u32& f, rsrc_t r, u32 off, u32 so) {
    asm volatile("v_mov_b32 %0, 0\n\tbuffer_atomic_add %0, %1, %2, %3 offen sc0 sc1" : "=&v"(f) : "v"(off), "s"(r), "s"(so) : "memory");
}
DEV void keep(u32 a, u32 b, u32 c) { asm volatile("" ::"v"(a), "v"(b), "v"(c)); }
DEV void wait_lds(lch* p, u32 want) {
#pragma unroll 1
    for (u32 it = 0; it < SPIN * 8; ++it) {
        u32 f = *(volatile LAS u32*)p;
        if (__builtin_amdgcn_readfirstlane(f) >= want) break;
        __builtin_amdgcn_s_sleep(1);
    }
    cbar();
}
DEV void sig_lds(lch* p, int lane) {
    cbar();
    if (lane == 0) asm volatile("ds_add_u32 %0, %1" ::"v"((u32)(uintptr_t)p), "v"(1u) : "memory");
    cbar();
}
#define PB 2
#define PRIO(n) __builtin_amdgcn_s_setprio(n)
#define PRIM() do { if (X) PRIO(PB); else PRIO(1); } while (0)
#define TN(n, v) do { } while (0)
#define TC(n) do { } while (0)
#define TS(n) do { } while (0)
__global__ void __launch_bounds__(512, 2)
k_main(const u16* q, const u16* k, const u16* v, const u16* sk, const u16* hk, const u16* sd,
       const u16* wqkv, const u16* wo, u16* out, char* ws) {
    extern __shared__ __attribute__((aligned(16))) char smem_[];
    lch* const L = (lch*)smem_;
    const int tid = threadIdx.x, lane = tid & 63;
    const int wv = __builtin_amdgcn_readfirstlane(tid >> 6);
    const int w = blockIdx.x, hd = (w >> 3) & 15, pi = 2 * (w & 7) + (w >> 7);
    const int X = wv >> 2, wq = wv & 3, tau = pi + 16 * X, b = 4 * tau + wq;
    const int n0 = lane & 15, gq = lane >> 4;
    const u32 lb = lane * 16;
    const u32 l0 = lane == 0 ? 0u : BIG;
    const bool conv = w < 224;
    const rsrc_t rws = mkrs(ws, WS_END);
    const v4u ones = {0x3f803f80u, 0x3f803f80u, 0x3f803f80u, 0x3f803f80u};
    const f4 z4 = {0, 0, 0, 0};
    lch* const cnt = L + L_CNT;
    lch* const sc = L + L_SCR + wv * 1024;
    const u32 img = WS_IMG + (u32)hd * IP;
    auto hpos = [](int r, int gq, int pads) {
        const int c = r >> 2, grp = (gq & 2) + ((gq ^ c ^ (c >> 1)) & 1);
        return grp * (12 + pads) + c * 3 + (r & 3);
    };
    TS(0);
    TC(62);

    u32 tag;
    {
        const int G = w >> 5, tile = w & 31, cch = X, pp = wq;
        v4u kr, vr, qr, kh, h0, h1, wr, sr;
        v2u skw;
        u32 dd;
        {
            const u32 n = conv ? SD2 : 0u;
            const rsrc_t rk = mkrs(k, n), rv = mkrs(v, n), rh = mkrs(hk, n), rq = mkrs(q, SD2);
            const u32 ro = (u32)((lane >> 3) * 896 + (lane & 7) * 16), so = (u32)((64 * tile + 8 * wv) * 896 + G * 128);
            kr = ld16s<0>(rk, ro, so);
            vr = ld16s<0>(rv, ro, so);
            kh = ld16s<0>(rk, tid < 32 && tile ? ro : BIG, tile ? so - 3584 : 0u);
            skw = ld8s<0>(mkrs(sk, 3584), (u32)(((lane >> 4) & 1) * 32 + (lane & 3) * 8), (u32)(G * 512 + wv * 64));
            const u32 epoch = ld4<16>(rws, 0);
            qr = ld16s<0>(rq, conv ? ro : BIG, so);
            {
                const int r = lane >> 1, rg = r >> 3, j7 = (r & 7) == 7, j = j7 ? (pi >= 7 ? pi - 7 : pi) : r & 7;
                const int row = 28 * (j7 ? 14 + (pi >= 7) : pi) + (j < 4 ? 4 * rg + j : 12 + 3 * rg + j);
                const bool isw = wv < 6;
                const rsrc_t rw_ = mkrs(isw ? wqkv : wo, isw ? (pi < 14 ? 448u * 2688u : 0u) : 448u * 896u);
                wr = ld16s<0>(rw_, !isw && j7 ? BIG : (u32)(row * (isw ? 2688 : 896) + (lane & 1) * 16),
                              (u32)(hd * 56 + (wv & 1) * 32 + (isw ? (wv >> 1) * 896 : 0)));
            }
            h0 = ld16s<0>(rh, lb, (u32)(w * 8192 + wv * 1024));
            h1 = ld16<0>(rh, (tid & 255) ? (u32)(w * 8192 + wv * 1024 - 16) + lb : BIG);
            dd = ld4<0>(mkrs(sd, 896), (u32)w * 4);
            sr = ld16s<0>(rq, lane < 56 ? lb : BIG, (u32)(8 * w + wv) * 896);
            if (tid < 16) lw4(cnt + tid * 4, 0);
            if (tid >= 64 && tid < 128) lw16(L + L_OWNB + (lane >> 5) * USTR + 4096 + (lane & 31) * 16, v4u{0, 0, 0, 0});
            if (tid >= 128 && tid < 144) lw16(L + lane * HSTR, v4u{0, 0, 0, 0});
            tag = __builtin_amdgcn_readfirstlane(epoch) + 1u;
            asm volatile("" ::"s"(tag));
        }
        {
            lch* const KS = L + L_WO;
            auto swz = [](int r) { return ((r >> 1) & 1) | (((r >> 3) & 3) << 1); };
            const int r1 = 8 * wv + (lane >> 3), c1 = lane & 7;
            lw16(KS + (r1 + 4) * 128 + ((c1 ^ swz(r1 + 4)) << 4), kr);
            lw16(KS + 8704 + r1 * 128 + ((c1 ^ swz(r1)) << 4), vr);
            if (tid < 32) lw16(KS + r1 * 128 + ((c1 ^ swz(r1)) << 4), kh);
            lw16(KS + 16896 + r1 * 128 + ((c1 ^ swz(r1)) << 4), qr);
            wgbar();
            TS(24);
            const int r2 = 32 * (lane >> 5) + 8 * (lane & 3) + ((lane >> 2) & 3);
            lch* p0 = KS + r2 * 128 + ((wv ^ swz(r2)) << 4) + ((lane >> 4) & 1) * 8;
            lch* p2 = KS + (r2 + 8) * 128 + ((wv ^ swz(r2 + 8)) << 4) + ((lane >> 4) & 1) * 8;
            const v2u k0 = ltr(p0), k1 = ltr(p0 + 512), k2 = ltr(p2), v0 = ltr(p0 + 8704), v1 = ltr(p0 + 8704 + 512);
            const v2u q0 = ltr(p0 + 16896), q1 = ltr(p0 + 16896 + 512);
            lw16(sc + lb, wr);
            const float kf[11] = {bhi(k0.x), blo(k0.y), bhi(k0.y), blo(k1.x), bhi(k1.x), blo(k1.y),
                                  bhi(k1.y), blo(k2.x), bhi(k2.x), blo(k2.y), bhi(k2.y)};
            const float vf[8] = {blo(v0.x), bhi(v0.x), blo(v0.y), bhi(v0.y), blo(v1.x), bhi(v1.x), blo(v1.y), bhi(v1.y)};
            const float w0 = blo(skw.x), w1 = bhi(skw.x), w2 = blo(skw.y), w3 = bhi(skw.y);
            float r[8];
#pragma unroll
            for (int j = 0; j < 8; ++j) r[j] = fm(kf[j + 3], w0, fm(kf[j + 2], w1, fm(kf[j + 1], w2, kf[j] * w3))) * vf[j];
            const u32 to = conv ? (u32)(((lane >> 4) & 1) * 16384 + (lane >> 5) * 256 + n0 * 16) : BIG;
            const u32 ts = (u32)((8 * G + wv) * 32768 + tile * 512);
            st16s<16>(v4u{pk(r[7], r[6]), pk(r[5], r[4]), pk(r[3], r[2]), pk(r[1], r[0])}, rws, to, WS_KVT + ts);
            st16s<16>(cat(q0, q1), rws, to, WS_QT + ts);
            lch* tr = sc + (8 * gq + (n0 >> 2)) * 32 + (n0 & 3) * 8;
            v4u wf = cat(ltr(tr), ltr(tr + 128));
            const int hr = n0 + (n0 * 43 >> 7);
            u32 wo_ = lb;
            if (wv & 1) {
                wo_ = n0 < 12 ? (u32)hpos(hr, gq, wv == 3) * 16 : wv == 3 && n0 == 12 ? (u32)(gq * 13 + 12) * 16 : BIG;
                if (n0 >= 12) wf = ones;
            }
            const u32 wt = wv == 1 ? HQ : wv == 3 ? HK : wv == 5 ? HV : wv == 7 ? 1024u : wv == 6 ? 0u : (u32)wv * 512;
            st16s<16>(wf, rws, wv < 6 && pi >= 14 ? BIG : wo_, img + (wv < 6 ? pi * WSTEP : L_WO + pi * OSTEP) + wt);
            TS(27);
        }
        {
            const u32 d[8] = {h1.x, h1.y, h1.z, h1.w, h0.x, h0.y, h0.z, h0.w};
            u32 a[7];
#pragma unroll
            for (int j = 0; j < 7; ++j) a[j] = __builtin_amdgcn_alignbit(d[j + 1], d[j], 16);
            lch* p = L + cch * 8 * HSTR + 16 + pp * 1024 + lb;
#pragma unroll
            for (int s = 0; s < 4; ++s) {
                lw16(p + 2 * s * HSTR, v4u{d[4 - s], d[5 - s], d[6 - s], d[7 - s]});
                lw16(p + (2 * s + 1) * HSTR, v4u{a[3 - s], a[4 - s], a[5 - s], a[6 - s]});
            }
            float s1 = 0, s2 = 0;
#pragma unroll
            for (int j = 0; j < 4; ++j) { s1 = dot2(sr[j], 0x3f803f80u, s1); s2 = dot2(sr[j], sr[j], s2); }
            s1 = wave_sum(s1) * (1.f / 448);
            s2 = wave_sum(s2) * (1.f / 448);
            const float xr = __builtin_amdgcn_rsqf(fmaxf(s2 - s1 * s1, 0.f) + 1e-5f);
            if (lane == 0) lw8(cnt + 64 + wv * 8, v2u{(bc<u32>(xr) & ~255u) | (tag & 255u), (bc<u32>(-s1 * xr) & ~255u) | ((tag >> 8) & 255u)});
        }
        drain();
        arrive(tag, rws, conv ? l0 : BIG, WS_F1 + (u32)(8 * G + wv) * FP + tile * 4);
        TS(1);
        const u32 f1o = lane < 4 && conv ? (u32)lane * 4 : BIG, f1s = WS_F1 + (u32)(w >> 2) * FP + wv * 16;
        wgbar();
        if (wv == 7) {
            arrive(tag, rws, l0, img + 104 * 1024 + pi * 4);
            st16s<16>(lr16(cnt + 64 + (lane & 3) * 16), rws, lane < 4 ? lb : BIG, WS_ST + (u32)w * 64);
        }
        const u32 f1 = probe(rws, f1o, f1s);

        v4u wb[10];
        if (conv) {
            v4u ha[2][8], ub_[2][8];
            {
                lch* p = L + (cch * 8 + ((7 - n0) & 7)) * HSTR + ((n0 >> 3) + gq + 8 * pp) * 16;
#pragma unroll
                for (int c = 0; c < 8; ++c)
#pragma unroll
                    for (int e = 0; e < 2; ++e) ha[e][c] = lr16(p + c * 512 + e * 64);
            }
            wait_from(f1, rws, f1o, f1s, tag);
            TS(2);
            v2u qt[2];
            v4u st[2];
            {
                const u32 so = (u32)((w >> 1) * 16384 + wv * 2048 + (w & 1) * 32);
                const v4u u0 = ld16s<16>(rws, (u32)((lane >> 1) * 64 + (lane & 1) * 16), WS_KVT + so);
                u32 fw;
                if (wv == 7) fw = probe(rws, lane < 16 ? (u32)lane * 4 : BIG, img + 104 * 1024);
#pragma unroll
                for (int h = 0; h < 2; ++h) qt[h] = ld8s<16>(rws, (u32)((lane >> 1) * 64 + (lane & 1) * 8 + h * 16), WS_QT + so);
                lw16(L + L_OWNB + (lane & 1) * USTR + (255 - 32 * wv - (lane >> 1)) * 16, u0);
                if (wv == 7) wait_from(fw, rws, lane < 16 ? (u32)lane * 4 : BIG, img + 104 * 1024, tag);
            }
            lch* const ub = L + L_OWNB + cch * USTR + (2032 - 1792 + 64 * pp - 16 * n0 + 8 * gq) * 2;
            lch* const rw = L + L_HALO + wv * 1024 + (gq * 16 + (n0 ^ (gq * 4))) * 16;
            v2u u[2];
            auto ldst = [&]() {
#pragma unroll
                for (int h = 0; h < 2; ++h) st[h] = ld16s<16>(rws, 2 * lb + h * 16, WS_ST + (u32)wv * 2048);
            };
            auto finish = [&]() {
                const int col = lane >> 2, qd = lane & 3;
                lch* rr = L + L_HALO + wq * 8192 + (qd * 16 + (col ^ (qd * 4))) * 16;
                f4 p[8];
#pragma unroll
                for (int j = 0; j < 8; ++j) p[j] = bc<f4>(lr16(rr + j * 1024));
                sb();
#pragma unroll 1
                for (u32 it = 0; it < SPIN; ++it) {
                    const u32 t1 = tag >> 8;
                    const u32 bad = xor_or(st[0].x, tag, xor_or(st[0].z, tag, xor_or(st[1].x, tag, st[1].z ^ tag))) |
                                    xor_or(st[0].y, t1, xor_or(st[0].w, t1, xor_or(st[1].y, t1, st[1].w ^ t1)));
                    if (__all((bad & 255u) == 0)) break;
                    __builtin_amdgcn_s_sleep(4);
                    ldst();
                }
                st[0] &= ~255u;
                st[1] &= ~255u;
                f4 y[2], x[2];
#pragma unroll
                for (int h = 0; h < 2; ++h) {
                    const float d = h ? bhi(dd) : blo(dd);
                    y[h] = (p[4 * h] + p[4 * h + 1]) + (p[4 * h + 2] + p[4 * h + 3]);
                    y[h][0] = fm(bhi(u[h].y), d, y[h][0]);
                    y[h][1] = fm(blo(u[h].y), d, y[h][1]);
                    y[h][2] = fm(bhi(u[h].x), d, y[h][2]);
                    y[h][3] = fm(blo(u[h].x), d, y[h][3]);
                    const f4 qv = {blo(qt[h].x), bhi(qt[h].x), blo(qt[h].y), bhi(qt[h].y)};
#pragma unroll
                    for (int e = 0; e < 4; ++e)
                        x[h][e] = fm(fm(qv[e], bc<float>(st[e >> 1][2 * (e & 1)]), bc<float>(st[e >> 1][2 * (e & 1) + 1])), y[h][e], qv[e]);
                }
                const u32 o = (u32)(col * 14336 + qd * 16), so = (u32)(16 * wv * 14336 + w * 64);
                st16s<16>(v4u{pk(x[0][0], x[0][1]), pk(x[0][2], x[0][3]), pk(x[1][0], x[1][1]), pk(x[1][2], x[1][3])}, rws, o, WS_X1 + so);
                st16s<16>(v4u{pk(y[0][0], y[0][1]), pk(y[0][2], y[0][3]), pk(y[1][0], y[1][1]), pk(y[1][2], y[1][3])}, rws, o, WS_YT + so);
            };
            f4 acc[4];
            auto macs = [&](int part, int s0, int s1) {
#pragma unroll
                for (int s = s0; s < s1; ++s)
#pragma unroll
                    for (int e = 0; e < 2; ++e)
#pragma unroll
                        for (int t = 0; t < 4; ++t) {
                            const int c = 4 * part + t - s;
                            if (c >= 0) acc[t] = mma(ha[e][c], ub_[e][s], s + e ? acc[t] : z4);
                        }
            };
            wgbar();
            TS(3);
#pragma unroll
            for (int s = 0; s < 4; ++s)
#pragma unroll
                for (int e = 0; e < 2; ++e) ub_[e][s] = lr16(ub + (7 - s) * 512 + e * 64);
#pragma unroll
            for (int h = 0; h < 2; ++h) u[h] = lr8(L + L_OWNB + h * USTR + (2044 - 256 * wv - 4 * lane) * 2);
            ldst();
            sb();
            macs(0, 0, 4);
#pragma unroll
            for (int t = 0; t < 4; ++t) lw16(rw + t * 8192, bc<v4u>(acc[t]));
            if (X) {
#pragma unroll
                for (int j = 0; j < 10; ++j) wb[j] = ldk<16>(rws, lb, img + wq * 10240, j);
            }
            wgbar();
            if (X == 0) finish();
            sb();
            macs(1, 0, 3);
#pragma unroll
            for (int t = 0; t < 4; ++t) pin(acc[t]);
#pragma unroll
            for (int s = 4; s < 8; ++s)
#pragma unroll
                for (int e = 0; e < 2; ++e) ub_[e][s] = lr16(ub + (7 - s) * 512 + e * 64);
            if (X == 0) {
                drain();
                arrive(tag, rws, lane < 4 ? (u32)lane * 4 * FP : BIG, WS_F2 + (u32)(wv * 16 + w / 56) * FP + (w % 56) * 4);
                TS(4);
            } else {
#pragma unroll
                for (int j = 0; j < 10; ++j) lw16(L + wq * 10240 + j * 1024 + lb, wb[j]);
            }
#pragma unroll
            for (int j = 0; j < 8; ++j) wb[j] = ldk<16>(rws, lb, img + 40960 + wv * 8192, j);
            sb();
            macs(1, 3, 8);
#pragma unroll
            for (int t = 0; t < 4; ++t) pin(acc[t]);
            wgbar();
#pragma unroll
            for (int t = 0; t < 4; ++t) lw16(rw + t * 8192, bc<v4u>(acc[t]));
#pragma unroll
            for (int j = 0; j < 8; ++j) lw16(L + 40960 + wv * 8192 + j * 1024 + lb, wb[j]);
            wgbar();
            if (X) finish();
        } else {
            if (wv == 7) wait_flag(rws, lane < 16 ? (u32)lane * 4 : BIG, img + 104 * 1024, tag);
            wgbar();
#pragma unroll 1
            for (int h = 0; h < 13; ++h) lw16(L + (wv * 13 + h) * 1024 + lb, ld16s<16>(rws, lb, img + (u32)(wv * 13 + h) * 1024));
            wgbar();
        }
    }
    lch* const wl = L + lb;
    lch* const wh = L + hpos((n0 & 3) == 3 ? n0 - 1 : n0, gq, 0) * 16;
    lch* const whk = L + HK + ((n0 & 3) == 3 ? hpos(n0 & 12, gq, 1) - (n0 >> 2) * 3 + 12 : hpos(n0, gq, 1)) * 16;
    if (X) {
        sig_lds(cnt + 20, lane);
        if (wq < 3) {
            f4 c0 = z4, c1 = z4;
            lch* ph = wq == 1 ? whk : wh + (wq == 0 ? HQ : HV);
#pragma unroll
            for (int s = 0; s < 14; ++s) {
                c0 = mma(lr16(wl + wq * 1024 + s * WSTEP), ones, c0);
                c1 = mma(lr16(ph + s * WSTEP), ones, c1);
            }
            if (n0 == 0) {
                lw16(L + L_CS + wq * 128 + gq * 16, bc<v4u>(c0));
                lw16(L + L_CS + wq * 128 + 64 + gq * 16, bc<v4u>(c1));
            }
            sig_lds(cnt + 28, lane);
        }
        if (conv) {
            drain();
            arrive(tag, rws, lane < 4 ? (u32)lane * 4 * FP : BIG, WS_F2 + (u32)(wv * 16 + w / 56) * FP + (w % 56) * 4);
            TS(4);
        }
    }
    TS(5);

#define IDS()                                                                                              \
    int ln_ = lane0, wv_ = wv0, w_ = w0;                                                                   \
    asm volatile("" : "+v"(ln_)); spin(wv_); spin(w_);                                                     \
    const int lane = ln_, wv = wv_, w = w_, hd = (w >> 3) & 15, X = wv >> 2, wq = wv & 3;                  \
    const int tau = 2 * (w & 7) + (w >> 7) + 16 * X, b = 4 * tau + wq, n0 = lane & 15, gq = lane >> 4;     \
    const u32 lb = lane * 16;                                                                              \
    (void)hd, (void)b, (void)n0, (void)gq, (void)lb
    const int lane0 = lane, wv0 = wv, w0 = w;
    const u32 gt = (0x2000u | (tag & 0x1fffu) | (tag & 0x2000u) << 2) << 16;
    u32 kvo = WS_KV + (u32)(hd * 32 + tau) * KP + wq * 2048, fo, hs, f0, f1, f2;
    v4u qf;
    f4 c0, c1;
    v4u yo;
    auto kvproj = [&](auto first_, u32 lo, u32 so) {
        constexpr bool FIRST = decltype(first_)::value;
        constexpr int ly = FIRST ? 0 : 1, PC = FIRST ? 896 : 1024;
        IDS();
        lch* const sc = L + L_SCR + wv * 1024;
        lch* const t0 = sc + gq * 128 + (n0 >> 3) * 64 + (n0 & 3) * 16 + ((n0 >> 2) & 1) * 8;
        const int ch = 16 + 3 * gq + (n0 >> 2);
        lch* const t1 = sc + (ch >> 1) * 64 + (n0 & 3) * 16 + (ch & 1) * 8;
        v4u xf[16], wf[2][4];
        auto ytr = [&](v4u& x) {
            lw16(sc + lb, x);
            x = cat(ltr(t0), ltr(t1));
        };
        auto ldw = [&](int s, int j) {
            wf[s & 1][j] = lr16((j == 0 ? whk : j == 3 ? wh + HV : wl + j * 1024) + s * WSTEP);
        };
        const bool real = tau - 4 + wq >= 0;
        fo = lane < 4 && real ? (u32)lane * 4 : BIG;
        u32 as = kvo + 8192 - (u32)wq * 2044, vo = n0 * 64 + gq * 16 + 1024;
        hs = real ? kvo - (u32)(4 - wq) * KP - (u32)wq * 2048 : 0u;
        spin(as); spin(hs);
        f4 ak0, ak1, av0, av1, ag;
#pragma unroll 1
        for (u32 it = 0; it < SPIN; ++it) {
            pin(lo);
            PRIO(0);
#pragma unroll
            for (int j = 0; j < 16; ++j) {
                const int s = (j + 14) & 15;
                xf[s] = ld16s<16>(rws, lo + (s & 3) * PC, so + (s >> 2) * 4 * PC);
                sb();
            }
            PRIM();
            if (FIRST && w == 0 && wv == 0) st4<16>(tag, rws, lane ? BIG : 0u);
#pragma unroll
            for (int j = 0; j < 4; ++j) ldw(0, j);
            u32 bad = 0;
            if (FIRST) {
                ytr(xf[14]);
                ytr(xf[15]);
                ytr(xf[0]);
                ytr(xf[1]);
            } else {
                bad = xor_or(xf[14].w, gt, xf[15].w ^ gt);
            }
            sb();
#pragma unroll
            for (int s = 0; s < 14; ++s) {
                if (!FIRST) bad = xor_or(xf[s].w, gt, bad);
                xf[s].w = __builtin_amdgcn_perm(xf[14 + s / 7][(s % 7) >> 1], xf[s].w, (s % 7) & 1 ? 0x07060100u : 0x05040100u);
                if (FIRST && s < 12) ytr(xf[s + 2]);
                sb();
                auto slot = [&](int j) {
                    if (s < 13) ldw(s + 1, j);
                    sb();
                };
                ag = mma(xf[s], xf[s], s ? ag : z4);
                slot(0);
                ak1 = mma(wf[s & 1][0], xf[s], s ? ak1 : z4);
                slot(1);
                ak0 = mma(wf[s & 1][1], xf[s], s ? ak0 : z4);
                slot(2);
                av0 = mma(wf[s & 1][2], xf[s], s ? av0 : z4);
                slot(3);
                av1 = mma(wf[s & 1][3], xf[s], s ? av1 : z4);
                sb();
            }
            if (FIRST || __all(bad <= 0xffffu)) break;
            TN(48, it + 1);
            __builtin_amdgcn_s_sleep(4);
        }
        TS(8 + 8 * ly);
        if (FIRST) wait_lds(cnt + 28, 3);
        PRIO(3);
        arrive(tag, rws, lane ? BIG : 0u, as);
        probe_(f0, rws, fo, hs + 8192);
        sb();
        float nm, r, tk[7], tv[7];
        {
            const f4 ck0 = bc<f4>(lr16(L + L_CS + 128 + gq * 16)), ck1 = bc<f4>(lr16(L + L_CS + 192 + gq * 16));
            const f4 cv0 = bc<f4>(lr16(L + L_CS + 256 + gq * 16)), cv1 = bc<f4>(lr16(L + L_CS + 320 + gq * 16));
            nm = ak1[3] * (-1.f / 448);
            const float d = (n0 & 2) ? ((n0 & 1) ? ag[3] : ag[2]) : ((n0 & 1) ? ag[1] : ag[0]);
            r = __builtin_amdgcn_rsqf(fmaxf(fm(-nm, nm, rows_sum(gq == (n0 >> 2) ? d : 0.f) * (1.f / 448)), 0.f) + 1e-5f);
#pragma unroll
            for (int e = 0; e < 7; ++e) {
                tk[e] = (e < 4 ? fm(nm, ck0[e], ak0[e]) : fm(nm, ck1[e - 4], ak1[e - 4])) * r;
                tv[e] = (e < 4 ? fm(nm, cv0[e], av0[e]) : fm(nm, cv1[e - 4], av1[e - 4])) * r;
            }
        }
        v4u kf = {pk(tk[0], tk[1]), pk(tk[2], tk[3]), pk(tk[4], tk[5]), pk(tk[6], bc<float>(gt))};
        st16s<16>(kf, rws, lb, kvo);
        v4u vf = {pk(tv[0], tv[1]), pk(tv[2], tv[3]), pk(tv[4], tv[5]), pk(tv[6], bc<float>(gt))};
        st16s<16>(vf, rws, vo, kvo);
        probe_(f1, rws, fo, hs + 8192);
        PRIM();
        sb();
        TS(36 + 8 * ly);
        {
            v4u qw[8];
            auto ldq = [&](int j) { qw[j & 7] = lr16(((j & 1) ? wh + HQ : wl) + (j >> 1) * WSTEP); };
#pragma unroll
            for (int j = 0; j < 8; ++j) ldq(j);
            if (ly) wait_lds(cnt + 16 + X * 4, 4 + 4 * X);
            lch* ob = L + (X ? L_OWNB + (3 - wq) * 2048 : L_OWNA + wq * 2048);
            kf.w &= 0xffffu;
            lw16(ob + lb, kf);
            lw8(ob + 1024 + n0 * 32 + gq * 8, v2u{vf.x, vf.y});
            lw8(ob + 1536 + n0 * 32 + gq * 8, v2u{vf.z, (vf.w & 0xffffu) | 0x3f800000u});
            const f4 cq0 = bc<f4>(lr16(L + L_CS + gq * 16)), cq1 = bc<f4>(lr16(L + L_CS + 64 + gq * 16));
            sb();
            f4 aq[2];
#pragma unroll
            for (int j = 0; j < 28; ++j) {
                aq[j & 1] = mma(qw[j & 7], xf[j >> 1], j > 1 ? aq[j & 1] : z4);
                if (j + 8 < 28) ldq(j + 8);
                if (j == 10) probe_(f2, rws, fo, hs + 8192);
                sb();
            }
            TS(9 + 8 * ly);
            const float rq = r * (0.18898223650461362f * 1.4426950408889634f);
            float t[7];
#pragma unroll
            for (int e = 0; e < 7; ++e) t[e] = (e < 4 ? fm(nm, cq0[e], aq[0][e]) : fm(nm, cq1[e - 4], aq[1][e - 4])) * rq;
            qf = v4u{pk(t[0], t[1]), pk(t[2], t[3]), pk(t[4], t[5]), pk(t[6], 1.f)};
            pin(qf);
        }
    };

    wait_flag(rws, lane < 56 ? (u32)lane * 4 : BIG, WS_F2 + (u32)b * FP, tag);
    sig_lds(cnt + X * 4, lane);
    wait_lds(cnt + X * 4, 4);
    TS(6);
    spin(kvo);
    kvproj(std::true_type{}, lane < 56 ? lb : BIG, WS_X1 + (u32)b * 14336);
#pragma unroll 1
    for (int ly = 0;; ++ly) {
        u32 oo, po, ao_;
        {
            IDS();
            const bool real = tau - 4 + wq >= 0;
            lch* ak = L + (X ? L_OWNB + (3 - wq) * 2048 : L_HALO + wq * 2048) + lb;
            lch* av = ak + 1024 - lane * 8;
            lch* hp = L + L_HALO + (X ? 3 - wq : wq) * 8192 + lb;
            lch* hv = hp + 1024 - lane * 8;
            const int dl = n0 - 4 * gq;
            f4 mf, ml;
#pragma unroll
            for (int e = 0; e < 4; ++e) {
                mf[e] = (e > dl) != (bool)X ? 0.f : -1e30f;
                ml[e] = (e > dl) == (bool)X ? 0.f : -1e30f;
            }
            u32 kpad = real ? 0u : 0xf1490000u, hl = real ? lb : BIG, hx = X ? 6144 : 0, gh = real ? gt : 0u;
            oo = WS_O + (u32)(ly * 128 + b) * XP;
            po = lane < 16 && lane != hd ? (u32)(hd & 3) * 256 + lane * 4 : BIG;
            ao_ = lane < 4 ? (u32)lane * 256 + hd * 4 : BIG;
            u32 os = oo + hd * 1024, lb2 = lb;
            lch* c_kv = cnt + 8 + X * 4;
            asm volatile("" : "+v"(mf), "+v"(ml), "+v"(ak), "+v"(av), "+v"(hp), "+v"(hv), "+v"(po), "+v"(ao_), "+v"(c_kv), "+v"(lb2));
            spin(os); spin(oo); spin(kpad); spin(hx); spin(gh);
            PRIO(3);
            wait3<0>(f0, f1, f2, rws, fo, hs + 8192, tag);
            TS(10 + 8 * ly);
            v4u hb[8];
            v4u xo;
#pragma unroll 1
            for (u32 it = 0; it < SPIN; ++it) {
                pin(hl);
#pragma unroll
                for (int j = 0; j < 8; ++j) hb[j] = ld16s<16>(rws, hl + (j & 1) * 1024, hs + ((j >> 1) * 2048 ^ hx));
                u32 bad = 0;
#pragma unroll
                for (int j = 0; j < 8; ++j) bad = xor_or(hb[j].w, gh, bad);
                if (__all(bad <= 0xffffu)) break;
                TN(49 + ly, it + 1);
                __builtin_amdgcn_s_sleep(4);
            }
            keep(f0, f1, f2);
            PRIM();
            TS(38 + 8 * ly);
            if (ly == 0) {
                xo = ld16s<16>(rws, lane < 56 ? lb : BIG, WS_X1 + (u32)b * 14336 + hd * 896);
                yo = ld16s<16>(rws, lane < 56 ? lb : BIG, WS_YT + (u32)b * 14336 + hd * 896);
            }
            wait_lds(cnt + 16 + (X ^ 1) * 4, 4 * (ly + 1));
#pragma unroll
            for (int j = 0; j < 4; ++j) {
                hb[2 * j].w = (hb[2 * j].w & 0xffffu) | kpad;
                lw16(hp + j * 2048, hb[2 * j]);
                lw8(hv + j * 2048, v2u{hb[2 * j + 1].x, hb[2 * j + 1].y});
                lw8(hv + j * 2048 + 512, v2u{hb[2 * j + 1].z, (hb[2 * j + 1].w & 0xffffu) | 0x3f800000u});
            }
            sig_lds(c_kv, lane);
            wait_lds(c_kv, 4 * (ly + 1));
            TS(11 + 8 * ly);
            v4u kf[17];
#pragma unroll
            for (int u = 0; u < 17; ++u) kf[u] = lr16(ak + u * 2048);
            sb();
            f4 s[17];
            v2u vf[17][2];
            float mx;
#pragma unroll
            for (int u = 0; u < 17; ++u) {
                s[u] = mma(kf[u], qf, u == 0 ? mf : u == 16 ? ml : z4);
                vf[u][0] = ltr(av + u * 2048);
                vf[u][1] = ltr(av + u * 2048 + 512);
                if (u == 2) mx = fmaxf(fmaxf(s[0][0], s[0][1]), fmaxf(s[0][2], s[0][3]));
                if (u > 2) mx = fmaxf(fmaxf(fmaxf(fmaxf(mx, s[u - 2][0]), s[u - 2][1]), s[u - 2][2]), s[u - 2][3]);
                sb();
            }
#pragma unroll
            for (int u = 15; u < 17; ++u) mx = fmaxf(fmaxf(fmaxf(fmaxf(mx, s[u][0]), s[u][1]), s[u][2]), s[u][3]);
            sig_lds(c_kv + 8, lane);
            mx = rows_max(mx);
            auto pe = [&](int u) {
                return v2u{pk(__builtin_amdgcn_exp2f(s[u][0] - mx), __builtin_amdgcn_exp2f(s[u][1] - mx)),
                           pk(__builtin_amdgcn_exp2f(s[u][2] - mx), __builtin_amdgcn_exp2f(s[u][3] - mx))};
            };
            f4 o0, o1;
            {
                const v4u p = cat(pe(16), v2u{0, 0});
                o0 = mma(cat(vf[16][0], vf[16][0]), p, z4);
                o1 = mma(cat(vf[16][1], vf[16][1]), p, z4);
            }
            if (ly == 0) {
                lch* const sc = L + L_SCR + wv * 1024;
                lch* const t0 = sc + gq * 128 + (n0 >> 3) * 64 + (n0 & 3) * 16 + ((n0 >> 2) & 1) * 8;
                const int ch = 16 + 3 * gq + (n0 >> 2);
                lch* const t1 = sc + (ch >> 1) * 64 + (n0 & 3) * 16 + (ch & 1) * 8;
                lw16(sc + lb, xo);
                xo = cat(ltr(t0), ltr(t1));
                lw16(sc + lb, yo);
                yo = cat(ltr(t0), ltr(t1));
                c0 = f4{blo(xo.x), bhi(xo.x), blo(xo.y), bhi(xo.y)};
                c1 = f4{blo(xo.z), bhi(xo.z), blo(xo.w), 0.f};
            }
            sb();
#pragma unroll
            for (int u = 0; u < 8; ++u) {
                const v4u pf = cat(pe(2 * u), pe(2 * u + 1));
                o0 = mma(cat(vf[2 * u][0], vf[2 * u + 1][0]), pf, o0);
                o1 = mma(cat(vf[2 * u][1], vf[2 * u + 1][1]), pf, o1);
                if (u == 6) {
                    PRIO(3);
                    arrive(tag, rws, ao_, oo + 16384);
                    probe_(f0, rws, po, oo + 16384);
                }
                sb();
            }
            const float inv = __builtin_amdgcn_rcpf(o1[3]);
            o0 *= inv;
            o1 *= inv;
            st16s<16>(v4u{pk(o0[0], o0[1]), pk(o0[2], o0[3]), pk(o1[0], o1[1]), pk(o1[2], bc<float>(gt))}, rws, lb2, os);
            probe_(f1, rws, po, oo + 16384);
            sb();
            TS(12 + 8 * ly);
        }
        u32 ss;
        {
            IDS();
            lch* wp = L + L_WO + lb;
            const int hr = n0 < 12 ? n0 + (n0 * 43 >> 7) : 0;
            lch* wph = L + L_WO + 1024 + hpos(ly ? hr : (n0 & 3) == 3 ? n0 - 1 : n0, gq, 0) * 16;
            v4u wg[32];
            auto ldg = [&](int j) { wg[j] = lr16(((j & 1) ? wph : wp) + (j >> 1) * OSTEP); };
#pragma unroll
            for (int j = 0; j < 32; ++j) ldg(j);
            u32 lb2 = lb;
            ss = WS_S1 + (u32)b * SP;
            spin(ss);
            sb();
            probe_(f2, rws, po, oo + 16384);
            wait3<0>(f0, f1, f2, rws, po, oo + 16384, tag);
            TS(14 + 8 * ly);
            f4 a0, a1;
#pragma unroll 1
            for (u32 it = 0; it < SPIN; ++it) {
                pin(lb2);
                v4u of[16];
                PRIO(0);
#pragma unroll
                for (int s = 0; s < 16; ++s) of[s] = ldk<16>(rws, lb2, oo, s);
                sb();
                PRIM();
                u32 bad = 0;
#pragma unroll
                for (int s = 0; s < 16; ++s) {
                    bad = xor_or(of[s].w, gt, bad);
                    a0 = mma(wg[2 * s], of[s], s ? a0 : c0);
                    a1 = mma(wg[2 * s + 1], of[s], s ? a1 : c1);
                    sb();
                }
                if (__all(bad <= 0xffffu)) break;
                TN(51 + ly, it + 1);
                __builtin_amdgcn_s_sleep(4);
            }
            keep(f0, f1, f2);
            c0 = a0;
            c1 = a1;
            TS(33 + 8 * ly);
        }
        if (ly) break;
        {
            IDS();
            PRIO(3);
            arrive(tag, rws, ao_, ss + 4096);
            sb();
            const float s1 = rows_sum(((c0[0] + c0[1]) + (c0[2] + c0[3])) + ((c1[0] + c1[1]) + c1[2]));
            const float s2 = rows_sum(fm(c0[0], c0[0], fm(c0[1], c0[1], fm(c0[2], c0[2], c0[3] * c0[3]))) + fm(c1[0], c1[0], fm(c1[1], c1[1], c1[2] * c1[2])));
            st16s<16>(v4u{bc<u32>(s1), bc<u32>(s2), tag, tag}, rws, lane < 16 ? lb : BIG, ss + hd * 256);
            probe_(f0, rws, po, ss + 4096);
            sb();
            TS(34);
            u32 xs = WS_X3 + (u32)b * XP, lb2 = lb;
            spin(xs);
            kvo += 512 * KP;
            const float y[7] = {blo(yo.x), bhi(yo.x), blo(yo.y), bhi(yo.y), blo(yo.z), bhi(yo.z), blo(yo.w)};
            __builtin_amdgcn_s_sleep(4);
            probe_(f1, rws, po, ss + 4096);
            __builtin_amdgcn_s_sleep(4);
            probe_(f2, rws, po, ss + 4096);
            wait3<0>(f0, f1, f2, rws, po, ss + 4096, tag);
            TS(15);
            v4u sp[4];
#pragma unroll 1
            for (u32 it = 0; it < SPIN; ++it) {
                pin(lb2);
#pragma unroll
                for (int m = 0; m < 4; ++m) sp[m] = ld16s<16>(rws, lb2 + m * 1024, ss);
                if (__all(xor_or(sp[0].z, tag, xor_or(sp[1].z, tag, xor_or(sp[2].z, tag, sp[3].z ^ tag))) == 0)) break;
                TN(53, it + 1);
                __builtin_amdgcn_s_sleep(4);
            }
            keep(f0, f1, f2);
            arrive(tag, rws, ao_, xs + 16384);
            sb();
            const float m1 = rows_sum((bc<float>(sp[0].x) + bc<float>(sp[1].x)) + (bc<float>(sp[2].x) + bc<float>(sp[3].x))) * (1.f / 448);
            const float m2 = rows_sum((bc<float>(sp[0].y) + bc<float>(sp[1].y)) + (bc<float>(sp[2].y) + bc<float>(sp[3].y))) * (1.f / 448);
            const float xr = __builtin_amdgcn_rsqf(fmaxf(m2 - m1 * m1, 0.f) + 1e-5f), xc = -m1 * xr;
#pragma unroll
            for (int e = 0; e < 4; ++e) c0[e] = fm(fm(c0[e], xr, xc), y[e], c0[e]);
#pragma unroll
            for (int e = 0; e < 3; ++e) c1[e] = fm(fm(c1[e], xr, xc), y[4 + e], c1[e]);
            st16s<16>(v4u{pk(c0[0], c0[1]), pk(c0[2], c0[3]), pk(c1[0], c1[1]), pk(c1[2], bc<float>(gt))}, rws, lb2, xs + hd * 1024);
            probe_(f0, rws, po, xs + 16384);
            sb();
            TS(35);
            lch* const sc = L + L_SCR + wv * 1024 + n0 * 64;
#pragma unroll
            for (int e = 0; e < 3; ++e) lw4(sc + gq * 12 + e * 4, bc<u32>(c1[e]));
            c1 = bc<f4>(lr16(sc + (gq < 3 ? gq : 0) * 16));
            pin(c1);
            __builtin_amdgcn_s_sleep(2);
            probe_(f1, rws, po, xs + 16384);
            __builtin_amdgcn_s_sleep(4);
            probe_(f2, rws, po, xs + 16384);
            wait3<0>(f0, f1, f2, rws, po, xs + 16384, tag);
            TS(7);
            const u32 g0 = f0, g1 = f1, g2 = f2;
            kvproj(std::false_type{}, lb, xs);
            keep(g0, g1, g2);
        }
    }
    {
        IDS();
        const rsrc_t ro = mkrs(out, SD2);
        const u32 uo = n0 * 896 + gq * 8, us = b * 14336 + hd * 56;
        st8s<0>(v2u{pk(c0[0], c0[1]), pk(c0[2], c0[3])}, ro, uo, us);
        st8s<0>(v2u{pk(c1[0], c1[1]), pk(c1[2], c1[3])}, ro, gq < 3 ? uo + 32 : BIG, us);
        TS(23);
        TC(63);
    }
}
}

extern "C" void kernel_launch(void* const* d_in, const int* in_sizes, int n_in, void* d_out, int out_size,
                              void* d_ws, size_t ws_size, hipStream_t stream) {
    (void)in_sizes; (void)n_in; (void)out_size; (void)ws_size;
    (void)hipFuncSetAttribute((const void*)k_main, hipFuncAttributeMaxDynamicSharedMemorySize, (int)L_END);
    hipLaunchKernelGGL(k_main, dim3(256), dim3(512), L_END, stream, (const u16*)d_in[0], (const u16*)d_in[1],
                       (const u16*)d_in[2], (const u16*)d_in[3], (const u16*)d_in[4], (const u16*)d_in[5],
                       (const u16*)d_in[7], (const u16*)d_in[8], (u16*)d_out, (char*)d_ws);
}
```

```cpp
#include <hip/hip_runtime.h>
#include <stdint.h>
#include <type_traits>
#include <utility>

extern "C" __device__ int buf_swap_(int, __amdgpu_buffer_rsrc_t, int, int, int) __asm("llvm.amdgcn.raw.ptr.buffer.atomic.swap.i32");

namespace {
typedef unsigned short u16;
typedef unsigned u32;
typedef u32 v4u __attribute__((ext_vector_type(4)));
typedef u32 v2u __attribute__((ext_vector_type(2)));
typedef float f4 __attribute__((ext_vector_type(4)));
typedef float f2 __attribute__((ext_vector_type(2)));
typedef float f16 __attribute__((ext_vector_type(16)));
typedef short s4 __attribute__((ext_vector_type(4)));
typedef __bf16 bf8 __attribute__((ext_vector_type(8)));
typedef __bf16 bf2 __attribute__((ext_vector_type(2)));
#define LAS __attribute__((address_space(3)))
typedef LAS char lch;
typedef __amdgpu_buffer_rsrc_t rsrc_t;
#define DEV __device__ __forceinline__

constexpr u32 SD2 = 2048u * 448u * 2u;
constexpr u32 KSTEP = 3648, QSTEP = 1792, HV = 2048, HK = 2816;
constexpr u32 L_Q = 14 * KSTEP, L_WO = L_Q + 14 * QSTEP, L_SCR = L_WO + 16 * QSTEP, L_OWNB = L_SCR + 8192, L_HALO = L_OWNB + 8192,
              L_OWNA = L_HALO + 32768, L_CS = L_OWNA + 8192, L_CNT = L_CS + 384, L_END = L_CNT + 64;
constexpr u32 HSTR = 4160, USTR = 40960 + 64;
static_assert(L_END <= 163840 && 16 * HSTR <= L_WO && 104 * 1024 <= L_SCR + 2048 && L_Q <= 52 * 1024, "lds map");
constexpr u32 FP = 4352;
constexpr u32 XP = 17664, SP = 5376, KP = 8448, IP = 111 * 1024 + 256;
constexpr u32 KQ = 16640, SDT = 112 * KQ;
constexpr u32 WS_F1 = FP, WS_F2 = WS_F1 + 112 * FP, WS_ST = 0x168000, WS_KVT = 0x170000, WS_QT = WS_KVT + SDT, WS_X1 = WS_QT + SDT,
              WS_YT = WS_X1 + 128 * XP,
              WS_KV = WS_YT + 128 * XP, WS_IMG = WS_KV + 1024 * KP, WS_END = WS_IMG + 16 * IP;
static_assert(WS_F2 + 224 * FP <= WS_ST, "ws map");
constexpr u32 WS_TS = WS_END;
constexpr u32 BIG = 0x7ff00000u;
constexpr u32 SPIN = 1u << 13;

template <class F, int... I> DEV void sfor_(F f, std::integer_sequence<int, I...>) { (f(std::integral_constant<int, I>{}), ...); }
template <int N, class F> DEV void sfor(F f) { sfor_(f, std::make_integer_sequence<int, N>{}); }
template <class T, class F> DEV T bc(F f) { return __builtin_bit_cast(T, f); }
DEV rsrc_t mkrs(const void* p, u32 bytes) { return __builtin_amdgcn_make_buffer_rsrc((void*)p, 0, bytes, 0x00020000); }
template <int AUX> DEV v4u ld16(rsrc_t r, u32 o) { return __builtin_amdgcn_raw_buffer_load_b128(r, o, 0, AUX); }
template <int AUX> DEV u32 ld4(rsrc_t r, u32 o) { return __builtin_amdgcn_raw_buffer_load_b32(r, o, 0, AUX); }
template <int AUX> DEV void st4(u32 v, rsrc_t r, u32 o) { __builtin_amdgcn_raw_buffer_store_b32(v, r, o, 0, AUX); }
template <int AUX> DEV v4u ld16s(rsrc_t r, u32 o, u32 so) { return __builtin_amdgcn_raw_buffer_load_b128(r, o, so, AUX); }
template <int AUX> DEV v2u ld8s(rsrc_t r, u32 o, u32 so) { return __builtin_amdgcn_raw_buffer_load_b64(r, o, so, AUX); }
template <int AUX> DEV void st16s(v4u v, rsrc_t r, u32 o, u32 so) {
    __builtin_amdgcn_raw_buffer_store_b128(v, r, o, so, AUX);
    asm volatile("s_nop 1" ::"v"(v) : "memory");
}
template <int AUX> DEV void st8s(v2u v, rsrc_t r, u32 o, u32 so) { __builtin_amdgcn_raw_buffer_store_b64(v, r, o, so, AUX); }
template <int AUX> DEV v4u ldk(rsrc_t r, u32 lb, u32 so, int j) { return ld16s<AUX>(r, lb + (j & 3) * 1024, so + (j >> 2) * 4096); }
DEV void sb() { __builtin_amdgcn_sched_barrier(0); }
DEV f4 mma(v4u a, v4u b, f4 c) { return __builtin_amdgcn_mfma_f32_16x16x32_bf16(bc<bf8>(a), bc<bf8>(b), c, 0, 0, 0); }
DEV u32 pk(float lo, float hi) { f2 f = {lo, hi}; return bc<u32>(__builtin_convertvector(f, bf2)); }
DEV float blo(u32 d) { return bc<float>(d << 16); }
DEV float bhi(u32 d) { return bc<float>(d & 0xffff0000u); }
DEV float dot2(u32 a, u32 b, float c) { return __builtin_amdgcn_fdot2_f32_bf16(bc<bf2>(a), bc<bf2>(b), c, false); }
DEV u32 xor_or(u32 a, u32 b, u32 c) { return __builtin_amdgcn_bitop3_b32(a, b, c, 0xbe); }
template <class T> DEV void spin(T& v) { v = __builtin_amdgcn_readfirstlane(v); asm volatile("" : "+s"(v)); }
template <class T> DEV void pin(T& v) { asm volatile("" : "+v"(v)); }
DEV float pv(float v) { asm volatile("" : "+v"(v)); return v; }
DEV float fm(float a, float b, float c) { return __builtin_fmaf(a, b, c); }
DEV v4u lr16(lch* p) { return *(LAS v4u*)p; }
DEV v2u lr8(lch* p) { return *(LAS v2u*)p; }
DEV void lw16(lch* p, v4u v) { *(LAS v4u*)p = v; }
DEV void lw8(lch* p, v2u v) { *(LAS v2u*)p = v; }
DEV void lw4(lch* p, u32 v) { *(LAS u32*)p = v; }
DEV v2u ltr(lch* p) { return bc<v2u>(__builtin_amdgcn_ds_read_tr16_b64_v4i16((LAS s4*)p)); }
DEV v4u cat(v2u a, v2u b) { return v4u{a.x, a.y, b.x, b.y}; }
DEV void cbar() { asm volatile("" ::: "memory"); }
DEV void wgbar() { asm volatile("s_waitcnt lgkmcnt(0)\n\ts_barrier" ::: "memory"); }
DEV void drain() { asm volatile("s_waitcnt vmcnt(0)" ::: "memory"); }
DEV float rows_max(float v) {
    auto a = __builtin_amdgcn_permlane32_swap(bc<u32>(v), bc<u32>(v), false, false);
    v = fmaxf(bc<float>(a[0]), bc<float>(a[1]));
    auto b = __builtin_amdgcn_permlane16_swap(bc<u32>(v), bc<u32>(v), false, false);
    return fmaxf(bc<float>(b[0]), bc<float>(b[1]));
}
DEV float rows_sum(float v) {
    auto a = __builtin_amdgcn_permlane32_swap(bc<u32>(v), bc<u32>(v), false, false);
    v = bc<float>(a[0]) + bc<float>(a[1]);
    auto b = __builtin_amdgcn_permlane16_swap(bc<u32>(v), bc<u32>(v), false, false);
    return bc<float>(b[0]) + bc<float>(b[1]);
}
template <int R> DEV float ror_add(float v) { return v + bc<float>(__builtin_amdgcn_update_dpp(0u, bc<u32>(v), 0x120 + R, 0xf, 0xf, false)); }
DEV float wave_sum(float v) { return rows_sum(ror_add<1>(ror_add<2>(ror_add<4>(ror_add<8>(v))))); }
DEV void arrive(u32 tag, rsrc_t r, u32 o, u32 so) { (void)buf_swap_((int)tag, r, (int)o, (int)so, 16); }
DEV u32 probe(rsrc_t r, u32 off, u32 so) { return (u32)__builtin_amdgcn_raw_ptr_buffer_atomic_add_i32(0, r, off, so, 16); }
DEV void wait_from(u32 f, rsrc_t r, u32 off, u32 so, u32 tag) {
#pragma unroll 1
    for (u32 it = 0; it < SPIN; ++it) {
        off = f == tag ? BIG : off;
        if (__all(off == BIG)) break;
        __builtin_amdgcn_s_sleep(1);
        f = probe(r, off, so);
    }
    cbar();
}
DEV void wait_flag(rsrc_t r, u32 off, u32 so, u32 tag) { wait_from(probe(r, off, so), r, off, so, tag); }
#define PROBE_(F, O, R, S) "buffer_atomic_add " F ", " O ", " R ", " S " offen sc0 sc1\n\t"
template <int SLP, int Y = 1, int Z = 0> DEV void wait2(u32& f0, u32& f1, rsrc_t r, u32 off, u32 so, u32 tag, u32 cnt = SPIN) {
    unsigned long long msk = __ballot(off != BIG);
#define CK_(F, N) "v_cmp_ne_u32 vcc, %[tag], %[" F "]\n\ts_and_b64 %[msk], %[msk], vcc\n\ts_cbranch_scc0 2f\n\t"                          \
                  "s_sleep %[slp]\n\ts_mov_b64 exec, %[msk]\n\tv_mov_b32 %[" F "], 0\n\t" PROBE_("%[" F "]", "%[off]", "%[rs]", "%[so]") \
                  "s_mov_b64 exec, -1\n\ts_waitcnt vmcnt(" N ")\n\t"
#define IO_ : [f0] "+v"(f0), [f1] "+v"(f1), [cnt] "+s"(cnt), [msk] "+s"(msk)                                                      \
            : [tag] "s"(tag), [off] "v"(off), [rs] "s"(r), [so] "s"(so), [slp] "n"(SLP), [y] "n"(Y), [z] "n"(Z + 1)               \
            : "memory", "vcc", "scc"
    if (Z == 0)
        asm volatile("s_waitcnt vmcnt(%[y])\n1:\n\t" CK_("f0", "1") CK_("f1", "1") "s_sub_u32 %[cnt], %[cnt], 1\n\ts_cbranch_scc0 1b\n2:" IO_);
    else
        asm volatile("s_waitcnt vmcnt(%[y])\n\t" CK_("f0", "%[z]") "1:\n\t" CK_("f1", "1") CK_("f0", "1") "s_sub_u32 %[cnt], %[cnt], 1\n\ts_cbranch_scc0 1b\n2:" IO_);
#undef IO_
#undef CK_
}
DEV void probe_(u32& f, rsrc_t r, u32 off, u32 so) {
    f = 0;
    asm volatile(PROBE_("%0", "%1", "%2", "%3") : "+v"(f) : "v"(off), "s"(r), "s"(so) : "memory");
}
DEV void keep(u32 a, u32 b) { asm volatile("" ::"v"(a), "v"(b)); }
template <int SLP> DEV void meet(lch* p, u32 want) {
    u32 o = 1, c, cnt = SPIN * 8;
    asm volatile("s_mov_b64 exec, 1\n\tds_add_rtn_u32 %0, %3, %0\n\ts_mov_b64 exec, -1\n\ts_waitcnt lgkmcnt(0)\n\tv_readfirstlane_b32 %1, %0\n\t"
                 "s_add_u32 %1, %1, 1\n\ts_cmp_ge_u32 %1, %4\n\ts_cbranch_scc1 2f\n"
                 "1:\n\ts_sleep %5\n\tds_read_b32 %0, %3\n\ts_waitcnt lgkmcnt(0)\n\tv_readfirstlane_b32 %1, %0\n\ts_cmp_ge_u32 %1, %4\n\ts_cbranch_scc1 2f\n\t"
                 "s_sub_u32 %2, %2, 1\n\ts_cbranch_scc0 1b\n2:"
                 : "+v"(o), "=&s"(c), "+s"(cnt)
                 : "v"((u32)(uintptr_t)p), "s"(want), "n"(SLP)
                 : "memory", "scc");
}
DEV void wait_lds(lch* p, u32 want) {
#pragma unroll 1
    for (u32 it = 0; it < SPIN * 8; ++it) {
        u32 f = *(volatile LAS u32*)p;
        if (__builtin_amdgcn_readfirstlane(f) >= want) break;
        __builtin_amdgcn_s_sleep(1);
    }
    cbar();
}
DEV void sig_lds(lch* p, int lane) {
    cbar();
    if (lane == 0) asm volatile("ds_add_u32 %0, %1" ::"v"((u32)(uintptr_t)p), "v"(1u) : "memory");
    cbar();
}
DEV void bump(rsrc_t r, u32 o, u32 so) { (void)__builtin_amdgcn_raw_ptr_buffer_atomic_add_i32(1, r, o, so, 16); }
DEV bool last4(lch* p) {
    u32 o = 1;
    asm volatile("s_mov_b64 exec, 1\n\tds_add_rtn_u32 %0, %1, %0\n\ts_mov_b64 exec, -1\n\ts_waitcnt lgkmcnt(0)" : "+v"(o) : "v"((u32)(uintptr_t)p) : "memory");
    return __builtin_amdgcn_readfirstlane(o) == 3;
}
DEV void sig_all(lch* p) {
    asm volatile("s_mov_b64 exec, 1\n\tds_add_u32 %0, %1\n\ts_mov_b64 exec, -1" ::"v"((u32)(uintptr_t)p), "v"(1u) : "memory");
}
DEV void arrive1(u32 tag, rsrc_t r, u32 so) {
    asm volatile("s_mov_b64 exec, 1\n\tbuffer_atomic_swap %0, off, %1, %2 sc1\n\ts_mov_b64 exec, -1" ::"v"(tag), "s"(r), "s"(so) : "memory");
}
#define PRIO(n) __builtin_amdgcn_s_setprio(n)
#define COLD(c) __builtin_expect((c), 0)
DEV u32 lrv(lch* p) { return *(volatile LAS u32*)p; }
DEV void wait_lds2(u32 f, lch* p, u32 want) {
    if (COLD(__builtin_amdgcn_readfirstlane(f) < want)) wait_lds(p, want);
    cbar();
}
DEV void wait_after(u32 f, rsrc_t r, u32 off, u32 so, u32 tag) {
    if (COLD(__builtin_amdgcn_ballot_w64(f != tag && off != BIG) != 0)) wait_flag(r, f == tag ? BIG : off, so, tag);
    cbar();
}
template <int C, int B> DEV u32 dpm(u32 old, u32 v) { return __builtin_amdgcn_update_dpp(old, v, C, 0xf, B, false); }
template <int C> DEV u32 dpp(u32 old, u32 v) { return __builtin_amdgcn_update_dpp(old, v, C, 0xf, 0xf, false); }
#define TN(n, v) do { } while (0)
#define TQ(i) do { } while (0)
#define TF(i, n) do { } while (0)
#define TC(n) do { } while (0)
#define TS(n) do { } while (0)
#define PA 0x0707
#define PB 0
template <int N> DEV void prio1(u32 m) {
    if (((PA | PB) & (0x101 << N)) == 0) PRIO(1);
    else asm volatile("s_setprio 1\n\ts_bitcmp0_b32 %0, %1\n\ts_cbranch_scc1 1f\n\ts_setprio 2\n1:" ::"s"(m), "n"(N) : "scc");
}
#define CODE 29100
#define H2 2
#define PZP 0
#define P0 19
#define P1 24
#define B0 0
#define B1 3
#define NCS 8
#define AGE 4
#define FS 2
#define QL 2
#define Q7(i) do { if (QL == 7) { sb(); TQ(i); sb(); } } while (0)
#define Q7F do { if (QL == 7) { TF(0, 13); TF(1, 21); TF(2, 29); TF(3, 30); TF(4, 31); TF(5, 32); TF(6, 39); TF(7, 40); } } while (0)
#define AB 5
#define EL 0
#define WA 13
#define NA 8
#define QB 11
#define SB 0
#define A1 4
#define M0 12
#define BP0 0
#define BP1 6
#define NB 0
#define PRA 1
#define R2 0
#define PB_ 13
#define LA 11
#define NOYA 0
#define HA -1
#define FW 0
#define WL 4
#define WW 27
#define QA 9
#define CA 1
#define PP 99
#define FQ 0
#define NFB 0
#define MS 0
#define YP 26
#define SS 3
#define UA 0
#define UB 0
#define TCH 0
#define TB 2
#define HL 0
#define FL 1
__global__ void __launch_bounds__(512, 2)
k_main(const u16* q, const u16* k, const u16* v, const u16* sk, const u16* hf, const u16* sd,
       const u16* wqkv, const u16* wo, u16* out, char* ws) {
    extern __shared__ __attribute__((aligned(16))) char smem_[];
    u32 tch;
    {
        const char* pc;
        asm volatile("s_getpc_b64 %0\n\tv_lshlrev_b32 %1, 7, %2\n\tv_cmp_gt_u32 vcc, %3, %1\n\tv_cndmask_b32 %1, 0, %1, vcc\n\tglobal_load_dword %1, %1, %0"
                     : "=&s"(pc), "=&v"(tch) : "v"(threadIdx.x), "n"(CODE) : "vcc");
    }
#define KEEPC asm volatile("" ::"v"(tch))
    lch* const L = (lch*)smem_;
    const int lane = threadIdx.x & 63;
    const int wv = __builtin_amdgcn_readfirstlane(threadIdx.x >> 6) ^ AGE;
    const int w = blockIdx.x, hd = (w >> 3) & 15, pi = 2 * (w & 7) + (w >> 7);
    const int X = wv >> 2, wq = wv & 3, tau = pi + 16 * X, b = 4 * tau + wq;
    const bool conv = w < 224;
    TS(0);
    const int G = w >> 5, tile = w & 31;
    u32 ro = (u32)((lane >> 3) * 896 + (lane & 7) * 16), so1 = (u32)((64 * tile + 16 * wq) * 896 + G * 128);
    const bool up_ = tile + wq > 0;
    u32 rh = lane >= 32 && up_ ? ro : BIG, soh = up_ ? so1 - 7168 : 0u, so2 = so1 + 7168, rs0 = (u32)((lane >> 3) * 32 + (lane & 3) * 8), sos = (u32)G * 512;
    u32 first = (u32)(w + X * 256);
    if (AB >= 4) lw4(L + L_CNT + (lane & 15) * 4, 0u);
    asm volatile("" : "+v"(ro), "+v"(rh), "+v"(rs0));
    asm volatile("" : "+s"(so1), "+s"(soh), "+s"(so2), "+s"(sos), "+s"(first));
    sb();
    if (AB >= 4) wgbar();
    sb();
    const rsrc_t rws = mkrs(ws, WS_END);
    v4u k0, k1, kh, v0, v1;
    v2u skw[2];
    u32 epoch0;
    if (first < 224) {
        PRIO(3);
        const rsrc_t rk = mkrs(k, SD2), rv = mkrs(v, SD2), rs_ = mkrs(sk, 3584);
        k0 = ld16s<0>(rk, ro, so1), k1 = ld16s<0>(rk, ro, so2), kh = ld16s<0>(rk, rh, soh);
        sb();
        v0 = ld16s<0>(rv, ro, so1), v1 = ld16s<0>(rv, ro, so2);
        sb();
#pragma unroll
        for (int i = 0; i < 2; ++i) skw[i] = ld8s<0>(rs_, rs0 + i * 256, sos);
        epoch0 = ld4<16>(rws, 0);
        sb();
        TS(42);
    }
    if (COLD(first - 224 < 32)) {
        PRIO(3);
        const rsrc_t rq = mkrs(q, SD2);
        epoch0 = ld4<16>(rws, 0);
        v4u r[2][7];
#pragma unroll
        for (int j = 0; j < 14; ++j) r[j / 7][j % 7] = ld16s<0>(rq, ro + (j % 7) * 128, (j < 7 ? so1 : so2) - 896);
        sb();
        const u32 row = (u32)(64 * tile + 16 * wq);
        u32 tag = __builtin_amdgcn_readfirstlane(epoch0) + 1u;
        asm volatile("" : "+s"(tag));
        KEEPC;
#pragma unroll
        for (int h = 0; h < 2; ++h) {
            float s1 = 0, s2 = 0;
#pragma unroll
            for (int j = 0; j < 28; ++j) s1 = dot2(r[h][j >> 2][j & 3], 0x3f803f80u, s1), s2 = dot2(r[h][j >> 2][j & 3], r[h][j >> 2][j & 3], s2);
            auto sum8 = [](float s) {
                s += bc<float>(dpp<0xb1>(0, bc<u32>(s)));
                s += bc<float>(dpp<0x4e>(0, bc<u32>(s)));
                return (s + bc<float>(dpp<0x141>(0, bc<u32>(s)))) * (1.f / 448);
            };
            s1 = sum8(s1);
            s2 = sum8(s2);
            const float xr = __builtin_amdgcn_rsqf(fmaxf(s2 - s1 * s1, 0.f) + 1e-5f);
            st8s<16>(v2u{(bc<u32>(xr) & ~255u) | (tag & 255u), (bc<u32>(-s1 * xr) & ~255u) | ((tag >> 8) & 255u)}, rws,
                     lane & 7 ? BIG : (u32)((lane & 16) * 64 + (lane >> 5) * 16 + (lane & 8)), WS_ST + (row >> 8) * 2048 + (row & 255) * 4 + h * 32);
        }
    }
    cbar();
    const int n0 = lane & 15, gq = lane >> 4;
    u32 lb = lane * 16;
    pin(lb);
    const v4u ones = {0x3f803f80u, 0x3f803f80u, 0x3f803f80u, 0x3f803f80u};
    const f4 z4 = {0, 0, 0, 0};
    lch* const cnt = L + L_CNT;
    const u32 img = WS_IMG + (u32)hd * IP;
    auto hpos = [](int r, int gq, int pads) {
        const int c = r >> 2, grp = (gq & 2) + ((gq ^ c ^ (c >> 1)) & 1);
        return grp * (12 + pads) + c * 3 + (r & 3);
    };
    auto hrow = [](int n) { return n < 8 ? (n & 6) + n : 4 * n - 30; };
    TC(62);
    TN(57, __builtin_amdgcn_s_getreg(4 | 31 << 11));

    u32 tag, f0, f1;
    lch* const wl = L + lb;
    lch *wh, *whk;
    u32 f2o, f2s, po, ao_, fo, hl, vo, l56, tv_, ep;
    u32 kvo, as, hs, hf_, hx, xt, ss, xs, gt, gh, kpad, lk, lh, lo_, lsc, us, pm, wos, sx[16];
    auto pre = [&]() {
        cbar();
        wh = L + hpos((n0 & 3) == 3 ? n0 - 1 : n0, gq, 0) * 16;
        whk = L + HK + ((n0 & 3) == 3 ? hpos(n0 & 12, gq, 1) - (n0 >> 2) * 3 + 12 : hpos(n0, gq, 1)) * 16;
        lsc = L_SCR + (wv ^ 4) * 1024;
        const bool real = tau - 4 + wq >= 0;
        f2o = lane < 7 && wq == 3 ? (u32)lane * FP : BIG;
        f2s = WS_F2 + (u32)tau * 7 * FP;
        po = lane < 16 && lane != hd ? (u32)(hd & 3) * 256 + lane * 4 : BIG;
        ao_ = lane < 4 ? (u32)lane * 256 + hd * 4 : BIG;
        fo = lane < 4 && real ? (u32)lane * 4 : BIG;
        hl = real ? lb : BIG;
        vo = n0 * 64 + gq * 16;
        l56 = (n0 < 8 ? gq * 128 + n0 * 16 : (n0 & 12) * 64 + (gq >> (n0 >> 2 & 1)) * 64 + (n0 & 3) * 16) + WS_X1 + (u32)b * XP;
        tv_ = tag;
        ep = w + wq + lane == 0 && X ? 0u : BIG;
        us = (u32)(b * 14336 + hd * 56);
        kvo = WS_KV + (u32)(hd * 32 + tau) * KP;
        as = WS_KVT + (u32)((43 + (hd >> 1)) * KQ + ((hd & 1) * 32 + tau) * 256);
        hf_ = as - (u32)(4 - wq) * 256;
        as += wq * 4;
        hs = real ? kvo - (u32)(4 - wq) * KP : 0u;
        kvo += wq * 2048;
        hx = X ? 6144 : 0;
        wos = (u32)(hd * IP + L_WO + (3 - wq) * 7168);
        pm = X ? PB : PA;
        ss = WS_KVT + (u32)(b * SP + (b * 171 >> 9) * (KQ - 3 * SP));
        xs = WS_X1 + (u32)b * XP;
        xt = WS_KVT + (u32)((51 + tau) * KQ + wq * 256 - 896);
        gt = (0x80u + (tag & 0x1fffu)) << 16;
        gh = real ? gt : 0u;
        kpad = real ? 0u : 0xf1490000u;
        lk = X ? L_OWNB + (3 - wq) * 2048 : L_HALO + wq * 2048;
        lh = L_HALO + (X ? 3 - wq : wq) * 8192;
        lo_ = X ? lk : lk + 32768;
#pragma unroll
        for (int p = 0; p < 16; ++p) {
            sx[p] = (hd ^ p) << 10;
            asm volatile("" : "+s"(sx[p]));
        }
        asm volatile("" : "+v"(wh), "+v"(whk), "+v"(f2o), "+v"(po), "+v"(ao_), "+v"(fo), "+v"(hl), "+v"(vo), "+v"(l56), "+v"(tv_), "+v"(ep));
        asm volatile("" : "+s"(f2s), "+s"(kvo), "+s"(as), "+s"(hs), "+s"(hf_), "+s"(hx), "+s"(xt), "+s"(ss), "+s"(xs), "+s"(tag));
        asm volatile("" : "+s"(gt), "+s"(gh), "+s"(kpad), "+s"(lk), "+s"(lh), "+s"(lo_), "+s"(lsc), "+s"(us), "+s"(pm), "+s"(wos));
        sb();
    };
    {
        v4u ha[2][8], ub_[2][8], st[2];
        v2u qt[2], u[2];
        f4 acc[2];
        u32 dd;
        lch *hp, *ub, *rw, *rr, *up, *qp;
        u32 xo_, f2l, qs, f2a, xw;
        auto setup = [&]() {
            hp = L + (X * 8 + ((7 - n0) & 7)) * HSTR + ((n0 >> 3) + gq + 8 * wq) * 16;
            ub = L + L_OWNB + X * USTR + (2032 - 1792 + 64 * wq - 16 * n0 + 8 * gq) * 2;
            rw = L + L_HALO + wv * 1024 + (gq * 16 + (n0 ^ (gq * 4))) * 16;
            const int col = lane >> 2, qd = lane & 3;
            rr = L + L_HALO + wq * 8192 + (qd * 16 + (col ^ (qd * 4))) * 16;
            up = L + L_OWNB + (2044 - 256 * wv - 4 * lane) * 2;
            xo_ = (u32)(col * XP + qd * 16);
            qp = L + L_SCR + wq * 2048 + X * 1024 + (lane >> 1) * 32 + (lane & 1) * 8;
            f2l = lane < 4 ? (u32)lane * 7 * FP : BIG;
            qs = (u32)((w >> 1) * KQ + wv * 2048 + (w & 1) * 32);
            f2a = WS_F2 + (u32)(wv * 28 + G) * FP;
            xw = WS_X1 + (u32)(16 * wv * XP + (w + (w * 293 >> 12) * 2) * 64);
            asm volatile("" : "+v"(hp), "+v"(ub), "+v"(rw), "+v"(rr), "+v"(up), "+v"(xo_), "+v"(qp), "+v"(f2l));
            asm volatile("" : "+s"(qs), "+s"(f2a), "+s"(xw));
            sb();
        };
        auto ldh = [&](int c) { ha[0][c] = lr16(hp + c * 512), ha[1][c] = lr16(hp + c * 512 + 64); };
        auto ldu = [&](int s) { ub_[0][s] = lr16(ub + (7 - s) * 512), ub_[1][s] = lr16(ub + (7 - s) * 512 + 64); };
        auto ldqt = [&]() { qt[0] = lr8(qp), qt[1] = lr8(qp + 16); };
        auto ldst = [&]() {
#pragma unroll
            for (int h = 0; h < 2; ++h) st[h] = ld16s<16>(rws, lb + h * 1024, WS_ST + (u32)wv * 2048);
        };
        auto tilef = [&](int t, auto fill) {
#pragma unroll
            for (int i = 0; i <= t; ++i) {
                const int s = i;
                acc[t & 1] = mma(ha[0][t - s], ub_[0][s], i ? acc[t & 1] : z4);
                acc[t & 1] = mma(ha[1][t - s], ub_[1][s], acc[t & 1]);
                fill(i);
                sb();
            }
        };
        auto put = [&](int t) { lw16(rw + (t & 3) * 8192, bc<v4u>(acc[t & 1])); };
        auto ldhs = [&]() {
#pragma unroll
            for (int c = 0; c < 8; ++c) ldh(c);
            sb();
        };
        auto part0 = [&](auto fill) {
#pragma unroll
            for (int c = 0; c < 4; ++c) ldu(c);
            sb();
            fill(0);
            sb();
            tilef(0, [&](int) { fill(1); });
            tilef(1, [&](int i) { if (!i) put(0); fill(2 + i); });
            tilef(2, [&](int i) { if (!i) put(1); fill(4 + i); });
            tilef(3, [&](int i) { if (!i) put(2); fill(7 + i); });
            put(3);
        };
        v4u wb[13];
        f4 gq_[2], qv[2], ud[2];
        auto chk = [&]() {
            const u32 t1_ = tag >> 8;
            auto bad = [&]() {
                return __builtin_amdgcn_ballot_w64(((xor_or(st[0].x, tag, xor_or(st[0].z, tag, xor_or(st[1].x, tag, st[1].z ^ tag))) |
                                                     xor_or(st[0].y, t1_, xor_or(st[0].w, t1_, xor_or(st[1].y, t1_, st[1].w ^ t1_)))) & 255u) != 0) != 0;
            };
            if (COLD(bad())) {
#pragma unroll 1
                for (u32 it = 0; it < SPIN; ++it) {
                    __builtin_amdgcn_s_sleep(4);
                    cbar();
                    ldst();
                    if (!bad()) break;
                }
                TN(54, 1);
            }
            st[0] &= ~255u;
            st[1] &= ~255u;
            asm volatile("" : "+v"(st[0]), "+v"(st[1]));
        };
        auto prep = [&]() {
#pragma unroll
            for (int h = 0; h < 2; ++h) {
                const float d = h ? bhi(dd) : blo(dd);
                qv[h] = f4{blo(qt[h].x), bhi(qt[h].x), blo(qt[h].y), bhi(qt[h].y)};
                ud[h] = f4{bhi(u[h].y) * d, blo(u[h].y) * d, bhi(u[h].x) * d, blo(u[h].x) * d};
#pragma unroll
                for (int e = 0; e < 4; ++e) gq_[h][e] = fm(qv[h][e], bc<float>(st[e >> 1][2 * (e & 1)]), bc<float>(st[e >> 1][2 * (e & 1) + 1]));
                asm volatile("" : "+v"(qv[h]), "+v"(ud[h]), "+v"(gq_[h]));
            }
        };
        auto finish = [&](auto mid) {
            f4 p[8], y[2], x[2];
#pragma unroll
            for (int j = 0; j < 8; ++j) p[j] = bc<f4>(lr16(rr + j * 1024));
            sb();
            mid();
#pragma unroll
            for (int h = 0; h < 2; ++h) {
                y[h] = ((ud[h] + p[4 * h]) + p[4 * h + 1]) + (p[4 * h + 2] + p[4 * h + 3]);
#pragma unroll
                for (int e = 0; e < 4; ++e) x[h][e] = fm(gq_[h][e], y[h][e], qv[h][e]);
            }
            st16s<16>(v4u{pk(x[0][0], x[1][0]), pk(x[0][1], x[1][1]), pk(x[0][2], x[1][2]), pk(x[0][3], x[1][3])}, rws, xo_, xw);
            st16s<16>(v4u{pk(y[0][0], y[1][0]), pk(y[0][1], y[1][1]), pk(y[0][2], y[1][2]), pk(y[0][3], y[1][3])}, rws, xo_, xw + 128 * XP);
            sb();
        };
        auto poison1 = [&](int i) { st16s<16>(v4u{~0u, ~0u, ~0u, ~0u}, rws, xo_, xw + i * 128 * XP); };
        auto poison = [&]() {
            poison1(0);
            sb();
        };
        const u32 io = X ? 53248 + wq * 6144 : wq * 13312;
        auto image = [&](int n) {
            wgbar();
            TS(3);
            v4u t[13];
#pragma unroll
            for (int j = 0; j < n; ++j) t[j] = ldk<16>(rws, lb, img + io, j);
#pragma unroll
            for (int j = 0; j < n; ++j) lw16(L + io + j * 1024 + lb, t[j]);
            TS(25);
        };
        auto swz = [](int r) { return ((r >> 1) & 1) * 4 + ((r >> 3) & 1) * 2; };
        const u32 swo = (lane >> 3) * 128 + (((lane & 7) ^ swz(lane >> 3)) << 4);
        const u32 sro = ((n0 & 1) * 8 + (n0 >> 2)) * 128 + ((gq ^ swz((n0 & 1) * 8 + (n0 >> 2))) << 4) + (n0 & 2) * 4;
        const u32 to_ = (u32)((lane >> 3) * KQ + (lane & 7) * 16), ts_ = WS_KVT + (u32)(G * 16 * KQ + (tile * 8 + wq * 2) * 64);
        const u32 f1t_ = WS_F1 + (u32)(G * 4 + ((tile >> 2) & 3)) * 4 * FP + ((tile >> 4) * 4 + (tile & 3)) * 4;
        if (X == 0) {
            PRIO(3);
            if (conv) {
                const u32 epoch = epoch0;
                if (AB < 4) wgbar();
                lch* sw = L + L_WO + wq * 5120 + swo;
                lch* sw2 = L + L_WO + wq * 5120 + (swo ^ 32);
                lch* sr[4];
#pragma unroll
                for (int i = 0; i < 4; ++i) sr[i] = L + L_WO + wq * 5120 + (sro ^ (i * 32)), pin(sr[i]);
                u32 to = to_, ts = ts_, f1t = f1t_, f1a = lane < 4 ? (u32)lane * FP : BIG, ci = lane + wq ? BIG : WS_F2 + (u32)(tile * 7 + G) * FP;
                asm volatile("" : "+v"(sw), "+v"(sw2), "+v"(to), "+v"(f1a), "+v"(ci));
                asm volatile("" : "+s"(ts), "+s"(f1t));
                sb();
                lw16(sw2 + 1024, k0);
                KEEPC;
                sb();
                TS(43);
                lw16(sw + 2048, k1);
                lw16(sw, kh);
                v2u kd[2][3], vd[2][2];
#pragma unroll
                for (int i = 0; i < 2; ++i) kd[i][0] = ltr(sr[2 * i] + 512), kd[i][1] = ltr(sr[2 * i + 1] + 1024), kd[i][2] = ltr(sr[2 * i + 1] + 1536);
                lw16(sw + 3072, v0);
                lw16(sw2 + 4096, v1);
#pragma unroll
                for (int i = 0; i < 2; ++i) vd[i][0] = ltr(sr[2 * i] + 3072), vd[i][1] = ltr(sr[2 * i] + 3584);
                TS(24);
                sb();
#pragma unroll
                for (int i = 0; i < 2; ++i) {
                    const u32 wa = __builtin_amdgcn_alignbit(skw[i].x, skw[i].x, 16), wb_ = __builtin_amdgcn_alignbit(skw[i].y, skw[i].y, 16),
                              wc = skw[i].x & 0xffffu, wd = __builtin_amdgcn_perm(skw[i].x, skw[i].y, 0x07060100u), we = skw[i].y & 0xffff0000u;
                    u32 r[4];
#pragma unroll
                    for (int j = 0; j < 4; ++j) {
                        auto D = [&](int n) { return kd[i][n >> 1][n & 1]; };
                        const float ev = dot2(D(j + 2), wc, dot2(D(j + 1), wd, dot2(D(j), we, 0.f)));
                        const float od = dot2(D(j + 2), wa, dot2(D(j + 1), wb_, 0.f));
                        r[j] = pk(od * bhi(vd[i][j >> 1][j & 1]), ev * blo(vd[i][j >> 1][j & 1]));
                    }
                    st16s<16>(v4u{r[3], r[2], r[1], r[0]}, rws, to, ts + i * 8 * KQ);
                    sb();
                }
                tag = __builtin_amdgcn_readfirstlane(epoch) + 1u;
                st4<16>(tag * 64, rws, ci);
                TS(27);
                setup();
                lch* uw = L + L_OWNB + (lane & 1) * USTR + (127 - 32 * wq - (lane >> 1)) * 16;
                u32 f1o = lane < (FQ ? 16 : 8) ? (u32)lane * 4 : BIG, uo = (u32)((lane >> 1) * 64 + (lane & 1) * 16);
                u32 f1s = WS_F1 + (u32)((G * 4 + wq) * 4 + (w & 3)) * FP;
                asm volatile("" : "+v"(uw), "+v"(f1o), "+v"(uo));
                asm volatile("" : "+s"(f1s));
                drain();
                if (last4(cnt + 36)) arrive(tag, rws, f1a, f1t);
                dd = ld4<0>(mkrs(sd, 896), (u32)w * 4);
                probe_(f0, rws, f1o, f1s);
                TS(1);
                pre();
                probe_(f1, rws, f1o, f1s);
                wait2<FS>(f0, f1, rws, f1o, f1s, tag);
                const v4u u0 = ld16s<16>(rws, uo, WS_KVT + qs), u1 = ld16s<16>(rws, uo, WS_KVT + qs + 8192);
                v4u qa, qb;
                auto ldq = [&]() { qa = ld16s<16>(rws, uo, WS_QT + qs), qb = ld16s<16>(rws, uo, WS_QT + qs + 8192); };
                if (FQ) ldq();
                if (FQ < 2) ldst();
                u32 fq;
                if (!FQ) fq = probe(rws, f1o, f1s + 32);
                sb();
                TS(2);
                if (PZP == 0) poison();
                if (FQ == 2) ldst();
                wait_lds(cnt + 28, 4);
                ldhs();
                lch* iw = L + io + lb;
                u32 is = img + io;
                asm volatile("" : "+v"(iw));
                asm volatile("" : "+s"(is));
                if (PZP == 1) poison();
                lw16(uw + 2048, u0);
                lw16(uw, u1);
                keep(f0, f1);
                if (CA == 0) chk();
                if (!FQ) wait_after(fq, rws, f1o, f1s + 32, tag);
                if (PZP == 2) poison();
                if (!FQ) ldq();
                if (PZP == 4) poison();
                TS(28);
                wgbar();
                TS(3);
                if (PZP == 5) poison();
                part0([&](int i) {
                    if (i == 0) u[0] = lr8(up), u[1] = lr8(up + USTR);
                    if (i == 0 && NA) wb[0] = ldk<16>(rws, lb, is, 0), wb[1] = ldk<16>(rws, lb, is, 1);
                    if (2 + i < NA) wb[2 + i] = ldk<16>(rws, lb, is, 2 + i);
                    if (i == QA) lw16(wl + L_SCR + wq * 2048, qa), ldqt(), lw16(wl + L_SCR + wq * 2048 + 1024, qb);
                });
                Q7(0);
                if (CA == 1) chk();
                if (CA < 2) prep();
                TS(31);
                Q7(1);
                wgbar();
                Q7(2);
                TS(32);
                if (FQ == 1) asm volatile("s_waitcnt vmcnt(%0)" ::"n"(NA) : "memory");
                if (PZP < 4) bump(rws, f2l, f2a);
                finish([&]() {
                    if (CA == 2) chk(), prep(), sb();
                    wgbar();
                    if (CA == 3) chk(), prep(), sb();
                });
                TS(25);
                Q7(3);
#pragma unroll
                for (int s = 4; s < (UA ? 5 : 8); ++s) ldu(s);
                auto rest = [&]() {
#pragma unroll
                    for (int j = NA; j < 13; ++j) wb[j] = ldk<16>(rws, lb, is, j);
                    sb();
                };
                auto hint = [&]() {
                    asm volatile("s_waitcnt vmcnt(%0)" ::"n"(NA + 2) : "memory");
                    bump(rws, f2l, f2a);
                    sb();
                };
                static_assert(PZP < 4 || HA <= LA, "");
                if (PZP >= 4 && HA < 0) hint();
                if (LA < 0) rest();
                PRIO(PRA);
                auto sl = [&](int n) {
                    if (PZP >= 4 && n == HA) hint();
                    if (UA && n == 3) ldu(5);
                    if (UA && n == 8) ldu(6);
                    if (UA && n == 14) ldu(7);
                    if (NA && n == LA) rest();
                    if (!NA && n >= LA && n - LA < 13) wb[(n + 26 - LA) % 13] = ldk<16>(rws, lb, is, n - LA);
                    if (n >= M0 && n - M0 < 13 - WA) lw16(iw + (n - M0) * 1024, wb[(n + 26 - M0) % 13]);
                    if (n == P0) probe_(f0, rws, f2o, f2s);
                    if (n == P1) probe_(f1, rws, f2o, f2s);
                };
                tilef(4, [&](int i) { sl(i); });
                Q7(4);
                tilef(5, [&](int i) { if (i == 0) put(4); sl(5 + i); });
                Q7(5);
                tilef(6, [&](int i) { if (i == 0) put(5); sl(11 + i); });
                Q7(6);
                tilef(7, [&](int i) { if (i == 0) put(6); sl(18 + i); });
                Q7(7);
                put(7);
                Q7F;
                TS(26);
                static_assert(H2 >= 2 && PZP < 4 && NA > 0 && P0 <= 25, "");
                asm volatile("s_waitcnt vmcnt(%0)" ::"n"(13 - NA + 1 + (P1 <= 25)) : "memory");
                if (NFB) sig_all(cnt + 32);
                else wgbar();
                if (P1 > 25) probe_(f1, rws, f2o, f2s);
#pragma unroll
                for (int j = 13 - WA; j < 13; ++j) lw16(iw + j * 1024, wb[j]);
            } else {
                if (AB < 4) wgbar();
                tag = __builtin_amdgcn_readfirstlane(epoch0) + 1u;
                asm volatile("" : "+s"(tag));
                TS(1);
                PRIO(1);
                pre();
                image(13);
                probe_(f0, rws, f2o, f2s);
                wgbar();
                __builtin_amdgcn_s_sleep(A1);
                probe_(f1, rws, f2o, f2s);
            }
            sig_all(cnt);
            PRIO(3);
            TS(4);
            wait2<FS>(f0, f1, rws, f2o, f2s, tag * 64 + 32);
            TS(5);
            meet<MS>(cnt + 4, 4);
        } else {
            auto pads = [&]() {
                if (wq == 0) lw16(L + L_OWNB + (lane >> 5) * USTR + 4096 + (lane & 31) * 16, v4u{0, 0, 0, 0});
                if (AB < 4 && wq == 1 && lane < 20) lw16(lane < 16 ? L + lane * HSTR : cnt + (lane & 3) * 16, v4u{0, 0, 0, 0});
                if (AB >= 4 && wq == 1) lw16(L + n0 * HSTR, v4u{0, 0, 0, 0});
            };
            if (AB < 4) pads();
            if (AB == 3) wgbar();
            v4u wr[2], qr[2], hc[2], hz;
            const u32 fws = img + 111 * 1024;
            lch *const sc2 = L + L_SCR + wq * 2048, *tr, *sw, *sw2, *sr0, *sr1;
            u32 o0, o1, o2, s0, s1, to, ts;
            auto addr = [&]() {
                tr = sc2 + (8 * gq + (n0 >> 2)) * 64 + (n0 & 3) * 8;
                const int hr = hrow(n0);
                const bool off = wq < 3 && pi >= 14;
                o0 = off ? BIG : lb;
                o1 = off ? BIG : n0 < 12 ? (u32)hpos(hr, gq, wq == 1) * 16 : wq == 1 && n0 == 12 ? (u32)(gq * 13 + 12) * 16 : BIG;
                s0 = img + (wq == 0 ? L_Q + pi * QSTEP : wq == 3 ? L_WO + pi * QSTEP : pi * KSTEP + (wq - 1) * 1024);
                s1 = s0 + (wq == 1 ? HK : 1024u), to = conv ? to_ : BIG, ts = ts_ + SDT;
                o2 = off || wq == 3 || lane >= 32 ? BIG : img + 104 * 1024 + pi * 512 + wq * 128 + (gq ? (n0 < 12 ? 16 + hr : hr + 1) : n0) * 4;
                lch* const qb = L + L_HALO + wq * 2048;
                sw = qb + swo, sw2 = qb + (swo ^ 32), sr0 = qb + sro, sr1 = qb + (sro ^ 64);
                asm volatile("" : "+v"(tr), "+v"(o0), "+v"(o1), "+v"(o2), "+v"(to), "+v"(sw), "+v"(sw2), "+v"(sr0), "+v"(sr1));
                asm volatile("" : "+s"(s0), "+s"(s1), "+s"(ts));
                sb();
            };
            auto ldf = [&]() {
                const rsrc_t rh = mkrs(hf, SD2);
                const u32 so = (u32)(w * 8192 + wq * 2048);
                hc[0] = ld16s<0>(rh, lb, so);
                hz = ld16s<0>(rh, lane == 0 && (wq & 1) ? 0u : BIG, (wq & 1) ? so - 16 : 0u);
                hc[1] = ld16s<0>(rh, lb + 1024, so);
                dd = ld4<0>(mkrs(sd, 896), (u32)w * 4);
                sb();
            };
            u32 epoch;
            auto ldq = [&]() {
                epoch = ld4<16>(rws, 0);
                const rsrc_t rq = mkrs(q, conv ? SD2 : 0u);
                qr[0] = ld16s<0>(rq, ro, so1);
                qr[1] = ld16s<0>(rq, ro, so2);
                sb();
            };
            if (EL & 1) ldq();
            if (EL & 2) ldf();
            if (EL) cbar();
            {
                const int r = lane >> 2, rg = r >> 3, j7 = (r & 7) == 7, j = j7 ? (pi >= 7 ? pi - 7 : pi) : r & 7;
                const int js = 0x1224444 >> 4 * j & 7;
                const int row = 28 * ((j7 ? 14 + (pi >= 7) : pi) ^ hd) + rg * js + j + 24 - 6 * js;
                const bool isw = wq < 3;
                const rsrc_t rw_ = mkrs(isw ? wqkv : wo, isw ? (pi < 14 ? 448u * 2688u : 0u) : 448u * 896u);
                const int pw = isw ? 2688 : 896;
                u32 o[2];
#pragma unroll
                for (int i = 0; i < 2; ++i) o[i] = !isw && j7 ? BIG : (u32)((row + i * 2 * js) * pw + (lane & 3) * 16), pin(o[i]);
                if (AB == 0 || AB == 5) addr();
                TS(13);
                if (AB < 2) wgbar();
                TS(21);
#pragma unroll
                for (int i = 0; i < 2; ++i) wr[i] = ld16s<0>(rw_, o[i], (u32)(hd * 56 + (isw ? wq * 896 : 0)));
            }
            if (!(EL & 1)) ldq();
            sb();
            if (HL == 0 && !(EL & 2)) ldf();
            TS(42);
            if (AB == 2) wgbar();
            if (AB >= 4 && conv) pads();
            if (AB && AB != 5) addr();
            if (HL == 1) ldf();
            {
                TS(29);
                lw16(sc2 + lb, wr[0]);
                KEEPC;
                TS(43);
                lw16(sc2 + 1024 + lb, wr[1]);
                const v4u lo = cat(ltr(tr), ltr(tr + 256));
                v4u hi = cat(ltr(tr + 32), ltr(tr + 288));
                TS(30);
                lw16(sw, qr[0]);
                TS(24);
                lw16(sw2 + 1024, qr[1]);
                const v4u q0 = cat(ltr(sr0), ltr(sr0 + 512)), q1 = cat(ltr(sr1), ltr(sr1 + 512));
                const f4 sl = mma(ones, lo, z4), sh = mma(ones, hi, z4);
                st16s<16>(lo, rws, o0, s0);
                if (n0 >= 12) hi = ones;
                st16s<16>(hi, rws, o1, s1);
                st16s<16>(q0, rws, to, ts);
                st16s<16>(q1, rws, to, ts + 8 * KQ);
                st4<16>(bc<u32>(gq ? sh[0] : sl[0]), rws, o2);
                sb();
                if (HL == 2) ldf();
                TS(27);
            }
            tag = __builtin_amdgcn_readfirstlane(epoch) + 1u;
            setup();
            auto flag = [&](int n) {
                u32 fa = lane == 4 ? fws + pi * 4 : lane < 4 && conv ? f1t_ + 32 + lane * FP : BIG;
                pin(fa);
                asm volatile("s_waitcnt vmcnt(%0)" ::"n"(HL == 2 ? 4 : 0) : "memory");
                if (last4(cnt + 40)) arrive(tag, rws, fa, 0);
                TS(1);
            };
            if (FL == 2) pre();
            if (FL == 0 || FL == 2) flag(0);
#pragma unroll
            for (int c = 0; c < 2; ++c) {
                u32 d[8], a[7];
#pragma unroll
                for (int j = 0; j < 4; ++j) d[j] = dpp<0x138>(c ? dpp<0x13c>(0, hc[0][j]) : hz[j], hc[c][j]), d[4 + j] = hc[c][j];
#pragma unroll
                for (int j = 0; j < 7; ++j) a[j] = __builtin_amdgcn_alignbit(d[j + 1], d[j], 16);
                lch* p = L + (wq >> 1) * 8 * HSTR + 16 + (wq & 1) * 2048 + c * 1024 + lb;
#pragma unroll
                for (int s = 0; s < 4; ++s) {
                    lw16(p + 2 * s * HSTR, v4u{d[4 - s], d[5 - s], d[6 - s], d[7 - s]});
                    lw16(p + (2 * s + 1) * HSTR, v4u{a[3 - s], a[4 - s], a[5 - s], a[6 - s]});
                }
                if (c == 0 && FL == 1) flag(0);
                sb();
            }
            f4 cp[7];
            auto ldc = [&]() {
                if (wq == 2) {
#pragma unroll
                    for (int j = 0; j < 7; ++j) cp[j] = bc<f4>(ld16s<16>(rws, (lane & 31) < 24 ? lb : BIG, img + (104 + j) * 1024));
                }
                sb();
            };
            sig_all(cnt + 28);
            if (SB == -2) ldst();
            if (FL == 3) flag(0);
            if (FL != 2) pre();
            if (FL == 4) flag(0);
            TS(2);
            if (conv) {
                if (SB == -1) ldst();
                wait_lds(cnt + 28, 4);
                ldhs();
                if (SB < 0) chk();
            }
            if (wq == 3 && (NA || !conv)) wait_flag(rws, lane < 16 ? (u32)lane * 4 : BIG, fws, tag);
            TS(28);
            PRIO(1);
            if (conv) {
                lch* iw = L + io + lb;
                u32 is = img + io;
                asm volatile("" : "+v"(iw));
                asm volatile("" : "+s"(is));
                wgbar();
                TS(3);
                auto ld2 = [&]() {
#pragma unroll
                    for (int j = 0; j < 6; ++j) wb[j] = ldk<16>(rws, lb, is, j);
                    sb();
                };
                u32 fw;
                const u32 fwo = lane < 16 && wq == 3 ? (u32)lane * 4 : BIG;
                auto sl = [&](int n) {
                    if (!NA && n == FW) fw = probe(rws, fwo, fws);
                    if (n == PB_ || n == PB_ + 1) poison1(n - PB_);
                    if (UB && n == UB) ldu(4);
                    if (UB && n == 16) ldu(5);
                    if (UB && n == 20) ldu(6);
                    if (UB && n == 26) ldu(7);
                    if (n == QB) ldqt();
                    if (n == SB) ldst();
                    if (n == PP) chk(), prep();
                };
                part0([&](int i) { if (!UB && i >= 1 && i < 5) ldu(3 + i); sl(i); });
                Q7(0);
                TS(31);
                wgbar();
                Q7(1);
                TS(32);
                tilef(4, [&](int i) { if (i == 4) u[0] = lr8(up), u[1] = lr8(up + USTR); sl(11 + i); });
                if (!NA) wait_after(fw, rws, fwo, fws, tag);
                Q7(2);
                wgbar();
                Q7(3);
                tilef(5, [&](int i) { if (i == 0) put(4); sl(16 + i); });
                Q7(4);
                tilef(6, [&](int i) { if (i == 0) put(5); sl(22 + i); });
                Q7(5);
                tilef(7, [&](int i) { if (i == 0) put(6); sl(29 + i); });
                Q7(6);
                put(7);
                if (H2 == 0) bump(rws, f2l, f2a);
                if (R2 == 1) ld2();
                if (SB >= 0 && PP > 36) chk();
                if (PP > 36) prep();
                Q7(7);
                Q7F;
                TS(26);
                PRIO(3);
                asm volatile("s_waitcnt vmcnt(%0)" ::"n"(R2 == 1 ? 6 : 0) : "memory");
                if (H2 == 1) bump(rws, f2l, f2a);
                if (R2 == 2) ld2();
                ldc();
                if (NFB) meet<0>(cnt + 32, 8);
                else wgbar();
                if (H2 == 2) bump(rws, f2l, f2a);
                finish([]() {});
                if (H2 == 3) bump(rws, f2l, f2a);
                TS(25);
                sig_all(cnt + 20);
                if (R2) {
#pragma unroll
                    for (int j = 0; j < 6; ++j) lw16(iw + j * 1024, wb[j]);
                    sb();
                    __builtin_amdgcn_s_sleep(B0);
                    probe_(f0, rws, f2o, f2s);
                    __builtin_amdgcn_s_sleep(B1);
                    probe_(f1, rws, f2o, f2s);
                } else {
#pragma unroll
                    for (int j = 0; j < 6; ++j)
                        asm volatile("buffer_load_dwordx4 %0, %1, %2, %3 offen offset:%4 sc1" : "=v"(wb[j]) : "v"(lb), "s"(rws), "s"(is + (j >> 2) * 4096), "n"((j & 3) * 1024) : "memory");
                    sb();
                    __builtin_amdgcn_s_sleep(B0);
                    probe_(f0, rws, f2o, f2s);
                    __builtin_amdgcn_s_sleep(B1);
                    probe_(f1, rws, f2o, f2s);
#pragma unroll
                    for (int j = 0; j < 6; ++j)
                        asm volatile("s_waitcnt vmcnt(%2)\n\tds_write_b128 %0, %1 offset:%3" ::"v"((u32)(uintptr_t)iw), "v"(wb[j]), "n"(7 - j), "n"(j * 1024) : "memory");
                }
            } else {
                image(6);
                ldc();
                TS(26);
                wgbar();
                wait_lds(cnt + 4, 4);
                __builtin_amdgcn_s_sleep(NCS);
                PRIO(3);
                sig_all(cnt + 20);
                probe_(f0, rws, f2o, f2s);
                __builtin_amdgcn_s_sleep(B1);
                probe_(f1, rws, f2o, f2s);
            }
            TS(4);
            if (wq == 2) {
                f4 a = ((cp[0] + cp[1]) + (cp[2] + cp[3])) + ((cp[4] + cp[5]) + cp[6]);
#pragma unroll
                for (int e = 0; e < 4; ++e) {
                    auto r = __builtin_amdgcn_permlane32_swap(bc<u32>(a[e]), bc<u32>(a[e]), false, false);
                    a[e] = bc<float>(r[0]) + bc<float>(r[1]);
                }
                if (lane < 24) lw16(L + L_CS + lb, bc<v4u>(a));
            }
            TS(5);
            wait2<FS>(f0, f1, rws, f2o, f2s, tag * 64 + 32);
            meet<1>(cnt, 8);
        }
    }
#define NQ 8
#define NL0 12
#define NL1 10
#define NLO 16
#define HO 4
#define HS 14
#define HX 0
#define JW0 20
#define JW1 26
#define WG1 16
#define EP 1
#define HK0 0
#define HK1 2
#define HKS 13
    f4 c0, c1;
    f4 mf, ml;
    v4u yo, qf, wf[84], hb[8];
    u32 h4[4], co;
    lch *ak, *av, *hp, *hv, *ob, *ov, *c_kv, *c_ot, *cs, *ql, *qh;
    u32 want = 4;
    auto ldw = [&](int i) {
        const int s = i >> 2, j = i & 3, n = i - 56;
        wf[i] = i < 56 ? lr16((j == 0 ? whk : j == 3 ? wh + HV : wl + (j - 1) * 1024) + s * KSTEP)
                       : lr16(((n & 1) ? qh : ql) + (n >> 1) * QSTEP);
    };
    auto ldh_ = [&]() {
#pragma unroll
        for (int j = 0; j < 8; ++j) {
            hb[j] = ld16s<16>(rws, hl + (j >> 2) * 1024, h4[j & 3]);
            sb();
        }
    };
    auto lim = [](u32& it, u32& thr, u32 lo = 0xffffu) {
        thr = ++it < SPIN ? lo : ~0u;
        asm volatile("" : "+s"(it), "+s"(thr));
    };
    auto torn = [&](u32 bad, u32 thr) { return COLD(__builtin_amdgcn_ballot_w64(bad > thr) != 0); };
    auto kvproj = [&](auto first_, u32 lo, v4u own, u32 fl) {
        constexpr bool FIRST = decltype(first_)::value;
        constexpr int ly = FIRST ? 0 : 1, NL = FIRST ? NL0 : NL1, HN = FIRST ? HK0 : HK1;
        constexpr int ord[16] = {14, 0, 1, 2, 3, FIRST ? 15 : 4, FIRST ? 4 : 5, FIRST ? 5 : 6, FIRST ? 6 : 15, 7, 8, 9, 10, 11, 12, 13};
        v4u xf[16];
        auto ldx = [&](int j) {
            const int s = ord[j];
            if (FIRST || s) xf[s] = ld16s<16>(rws, lo, sx[s]);
            else xf[s] = own;
        };
        u32 c6, bad, pa = 0x05040100u, pb = 0x07060100u, pc = pa, ys = sx[0] + 128 * XP;
        v2u tt[2];
        auto tr = [&](v4u& x, int part, int k = 0) {
            v2u& t = tt[k];
            if (part == 0) {
                t = v2u{x.z, x.w};
                pin(t);
                x.z = dpm<0x108, 0x3>(x.z, x.x);
                x.w = dpm<0x108, 0x3>(x.w, x.y);
            } else if (part == 1) {
                bad = max(bad, t.x);
                x.x = dpm<0x118, 0xc>(x.x, t.x);
                x.y = dpm<0x118, 0xc>(x.y, t.y);
                pin(bad);
            } else if (part == 2) {
                t.x = x.w, t.y = x.y;
                asm volatile("" : "+v"(t.x), "+v"(t.y));
                x.w = dpm<0x104, 0x5>(x.w, x.z);
            } else {
                x.y = dpm<0x104, 0x5>(x.y, x.x);
                x.z = dpm<0x114, 0xa>(x.z, t.x);
                x.x = dpm<0x114, 0xa>(x.x, t.y);
            }
        };
        u32 dl_[4], dh_[4];
        f4 ak0, ak1, av0, av1, ag, ck0, ck1, cv0, cv1;
        float dg, nm, rs;
        auto fix = [&](int s) {
            if (!FIRST && s) bad = xor_or(xf[s].w, gt, bad);
            xf[s].w = __builtin_amdgcn_perm(xf[14 + s / 7][(s % 7) >> 1], xf[s].w, (s % 7) & 1 ? pb : s % 7 == 6 ? pc : pa);
        };
        auto step = [&](auto s_) {
            constexpr int s = s_;
            if (ly == QL && s == 0) TQ(1);
            if (ly == QL && s == 4) TQ(3);
            if (ly == QL && s == 7) TQ(4);
            if (ly == QL && s == 10) TQ(5);
            if (ly == QL && s == 13) TQ(6);
            auto slot = [&](int j) {
                const int i = s < 10 ? 26 + 3 * s + j : 56 + 3 * (s - 11) + j;
                if (s < 10 ? i >= 28 : s > 10 && i < 56 + NQ) ldw(i);
                sb();
            };
            ag = mma(xf[s], xf[s], s ? ag : z4);
            if (s == HKS) {
                PRIO(3);
                if (HN == 0) arrive1(tv_, rws, as);
                probe_(f0, rws, fo, hf_);
            }
            if (NL + s < 16) ldx((NL + s) & 15);
            if (FIRST && s == 9) st4<16>(tv_, rws, ep);
            if (FIRST && s == 10) {
                wait_lds2(c6, cnt, 8);
                yo = ld16s<16>(rws, l56, ys);
            }
            if (FIRST && s < 13) tr(xf[s + 1], 3);
            slot(0);
            ak1 = mma(wf[4 * s], xf[s], s ? ak1 : z4);
            if (FIRST) {
                if (s >= 2 && s < 6) tr(xf[15], s - 2, 1);
                if (s < 13) fix(s + 1);
            } else if (s == 6) {
                bad = xor_or(xf[15].w, gt, bad);
            }
            if (s == 10) ck0 = bc<f4>(lr16(cs + 128)), ck1 = bc<f4>(lr16(cs + 192));
            sb();
            ak0 = mma(wf[4 * s + 1], xf[s], s ? ak0 : z4);
            if (s == 10) cv0 = bc<f4>(lr16(cs + 256)), cv1 = bc<f4>(lr16(cs + 320));
            if (s == 13) {
                dg = 0.f;
#pragma unroll
                for (int e = 0; e < 4; ++e) dg = __builtin_amdgcn_inverse_ballot_w64((unsigned long long)dh_[e] << 32 | dl_[e]) ? ag[e] : dg;
                pin(dg);
            }
            if (FIRST && s < 12) tr(xf[s + 2], 0);
            slot(1);
            av0 = mma(wf[4 * s + 2], xf[s], s ? av0 : z4);
            if (!FIRST && s < 13) fix(s + 1);
            if (FIRST && s < 12) tr(xf[s + 2], 1);
            if (s == 13) {
                dg = rows_sum(dg);
                nm = ak1[3] * (-1.f / 448);
                asm volatile("" : "+v"(dg), "+v"(nm));
            }
            sb();
            av1 = mma(wf[4 * s + 3], xf[s], s ? av1 : z4);
            if (FIRST && s < 12) tr(xf[s + 2], 2);
            if (FIRST && s == 8) c6 = lrv(cs + (L_CNT - L_CS));
            if (s == 10) {
#pragma unroll
                for (int e = 0; e < 4; ++e) asm volatile("s_mov_b32 %0, %2\n\ts_mov_b32 %1, %3" : "=s"(dl_[e]), "=s"(dh_[e]) : "n"(0x00100001u << e), "n"(0x10000100u << e));
            }
            slot(2);
        };
        u32 it = 0, thr;
        if (FIRST) {
            pa += (vo & 16) * 0x202u >> 4, pb = pa + 0x02020000u, pc = vo & 16 ? pb : pa;
            cs = L + L_CS + (vo & 48);
            ql = wl + L_Q, qh = wh + L_Q + 1024;
            c_kv = cnt + 8 + (hx >> 11 & 1) * 4;
            asm volatile("" : "+v"(pa), "+v"(pb), "+v"(pc), "+v"(cs), "+v"(ql), "+v"(qh), "+v"(c_kv));
            asm volatile("" : "+s"(ys));
        }
#pragma unroll 1
        do {
            cbar();
            if (!FIRST) {
                probe_(f0, rws, po, fl);
                TS(35);
            }
            if (FIRST) {
                TS(6);
            } else {
                u32 cnt2 = it ? 0u : SPIN;
                asm volatile("" : "+s"(cnt2));
                lim(it, thr);
                PRIO(1);
#pragma unroll
                for (int i = 0; i < 28; ++i) ldw(i);
                PRIO(3);
                probe_(f1, rws, po, fl);
                wait2<0>(f0, f1, rws, po, fl, tag, cnt2);
                TS(7);
            }
#pragma unroll
            for (int j = 0; j < NL; ++j) {
                ldx(j);
                if (j == 2) PRIO(0);
                sb();
            }
            prio1<0>(pm);
            if (ly == QL) TQ(0);
            if (FIRST) {
#pragma unroll
                for (int i = 0; i < 28; ++i) ldw(i);
                lim(it, thr, ~1u);
                bad = 0;
                sb();
#pragma unroll
                for (int i = 0; i < 4; ++i) tr(xf[14], i);
                keep(f0, f1);
#pragma unroll
                for (int i = 0; i < 4; ++i) tr(xf[0], i);
                fix(0);
#pragma unroll
                for (int i = 0; i < 3; ++i) tr(xf[1], i);
            } else {
                fix(0);
                keep(f0, f1);
                bad = xf[14].w ^ gt;
            }
            sb();
            sfor<13>(step);
        } while (torn(bad, thr));
        TN(FIRST ? 47 : 48, it - 1);
        step(std::integral_constant<int, 13>{});
        TS(8 + 8 * ly);
        if (HN == 1) arrive1(tv_, rws, as);
        rs = __builtin_amdgcn_rsqf(fmaxf(fm(-nm, nm, dg * (1.f / 448)), 0.f) + 1e-5f);
        auto ln = [&](f4 c0_, f4 a0, f4 c1_, f4 a1, float last) {
            float t[7];
#pragma unroll
            for (int e = 0; e < 7; ++e) t[e] = e < 4 ? fm(nm, c0_[e], a0[e]) : fm(nm, c1_[e - 4], a1[e - 4]), pin(t[e]);
#pragma unroll
            for (int e = 0; e < 7; ++e) t[e] *= rs, pin(t[e]);
            return v4u{pk(t[0], t[1]), pk(t[2], t[3]), pk(t[4], t[5]), pk(t[6], last)};
        };
        v4u kf_ = ln(ck0, ak0, ck1, ak1, bc<float>(gt));
        st16s<16>(kf_, rws, lb, kvo);
        if (HN == 2) arrive1(tv_, rws, as);
        sb();
        v4u vf_ = ln(cv0, av0, cv1, av1, bc<float>(gt));
        st16s<16>(vf_, rws, vo + 1024, kvo);
        asm volatile("" ::"v"(av1));
        if (HN == 3) arrive1(tv_, rws, as);
        probe_(f1, rws, fo, hf_);
        prio1<1>(pm);
        sb();
        TS(36 + 8 * ly);
        f4 aq[2], cq0, cq1;
        u32 cb, wol;
        int dl;
        v4u wo_[7];
        sfor<28>([&](auto j_) {
            constexpr int j = j_;
            if (ly + 5 == QL && j < 16 && !(j & 1)) TQ(j >> 1);
            aq[j & 1] = mma(wf[56 + j], xf[j >> 1], j > 1 ? aq[j & 1] : z4);
            if (j + NQ < 28) ldw(56 + (j + NQ) % 28);
            if (FIRST) {
                if (j == 0) {
                    ob = L + lo_ + lb, ov = L + lo_ + (vo >> 1);
                    asm volatile("" : "+v"(ob), "+v"(ov));
                }
                if (j == WL - 1) wol = hx ? lb : BIG, pin(wol);
                if (j >= WL && j < WL + 7) wo_[(j + 28 - WL) % 7] = ldk<16>(rws, wol, wos + WS_IMG, j - WL);
                if (j == WW && hx) {
#pragma unroll
                    for (int i = 0; i < 7; ++i) lw16(wl + L_WO + (lo_ - L_OWNB) / 2 * 7 + i * 1024, wo_[i]);
                    sig_all(cnt + 24);
                }
                if (j == 2) {
                    ak = L + lk + lb, hp = L + lh + lb;
                    asm volatile("" : "+v"(ak), "+v"(hp));
                }
                if (j == 3) {
                    av = L + lk + (lb >> 1), hv = L + lh + (lb >> 1);
                    asm volatile("" : "+v"(av), "+v"(hv));
                }
                if (j == 4) {
                    c_ot = (lch*)(uintptr_t)((u32)(uintptr_t)c_kv ^ 4u);
                    dl = (int)((vo >> 4) & 12) + (int)(vo >> 8) - (int)((vo >> 4) & 3);
                    asm volatile("" : "+v"(c_ot), "+v"(dl));
                }
                if (j >= 5 && j < 9) {
                    const int e = (j - 5) & 3;
                    const bool in = (4 * e > dl) != (hx != 0);
                    mf[e] = in ? 0.f : -1e30f;
                    ml[e] = in ? -1e30f : 0.f;
                    asm volatile("" : "+v"(mf[e]), "+v"(ml[e]));
                }
                if (j == 9) {
                    c0 = f4{blo(xf[0].x), bhi(xf[0].x), blo(xf[0].y), bhi(xf[0].y)};
                    pin(c0);
                }
                if (j == 10) {
                    c1 = f4{blo(xf[0].z), bhi(xf[0].z), blo(xf[0].w), 0.f};
                    pin(c1);
                }
                if (j == YP && COLD(__builtin_amdgcn_ballot_w64(yo.x == ~0u) != 0)) {
#pragma unroll 1
                    for (u32 n = 0; n < SPIN; ++n) {
                        cbar();
                        yo = ld16s<16>(rws, l56, ys);
                        if (__builtin_amdgcn_ballot_w64(yo.x == ~0u) == 0) break;
                    }
                    TN(55, 1);
                }
            }
            if (j == 1) {
                kf_.w &= 0xffffu;
                vf_.w = (vf_.w & 0xffffu) | 0x3f800000u;
                asm volatile("" : "+v"(kf_), "+v"(vf_));
            }
            if (j == 12) cb = lrv(c_kv + 8);
            if (ly == QL && j == 14) TQ(7);
            if (ly == QL && j == 27) TQ(2);
            if (j == 2) {
#pragma unroll
                for (int i = 0; i < 4; ++i) h4[i] = hs + (i * 2048 ^ hx);
                asm volatile("" : "+s"(h4[0]), "+s"(h4[1]), "+s"(h4[2]), "+s"(h4[3]));
            }
            if (j == (FIRST ? JW0 : JW1)) {
                sb();
                PRIO(3);
                wait2<0, (HN ? 4 : 3) + (FIRST ? 7 : 0), FIRST ? 7 : 0>(f0, f1, rws, fo, hf_, tag);
                ldh_();
                prio1<1>(pm);
                TS(10 + 8 * ly);
            }
            if (j == 20) {
                wait_lds2(cb, c_kv + 8, want - 4 + (hx >> 9 & 4));
                lw16(ob, kf_);
            }
            if (j == 21) {
                cq0 = bc<f4>(lr16(cs)), cq1 = bc<f4>(lr16(cs + 64));
            }
            if (j == 22) lw8(ov + 1024, v2u{vf_.x, vf_.y});
            if (j == 23) lw8(ov + 1536, v2u{vf_.z, vf_.w});
            if (j == 27) co = lrv(c_ot + 8);
            if (j == 24) rs *= 0.18898223650461362f * 1.4426950408889634f, pin(rs);
            sb();
        });
        PRIO(3);
        qf = ln(cq0, aq[0], cq1, aq[1], 1.f);
        asm volatile("" : "+v"(qf), "+v"(yo) : "v"(aq[1]));
        TS(9 + 8 * ly);
        if (ly == QL || ly + 5 == QL) { TF(0, 13); TF(1, 21); TF(2, 29); TF(3, 30); TF(4, 31); TF(5, 32); TF(6, 39); TF(7, 40); }
    };

    kvproj(std::true_type{}, l56, ones, 0);
    int ly = 0;
#pragma unroll 1
    do {
        {
            u32 uo, uo2;
            u32 of_ = ss + 4096 + ly * 192, ol = lb + (lb < 896 ? xs + 128 * XP : xt + 128);
            asm volatile("" : "+v"(ol));
            asm volatile("" : "+s"(of_));
            v4u m1;
            asm volatile("v_mov_b64 %0, 0\n\tv_mov_b64 %1, 0\n\ts_mov_b32 exec_hi, 0\n\tv_mov_b64 %0, %2\n\ts_mov_b32 exec_hi, -1"
                         : "=&v"(m1.xy), "=&v"(m1.zw) : "s"(0xbf80bf80bf80ull));
            pin(vo);
            const int n0 = vo >> 6, gq = (vo >> 4) & 3;
            lch* wph = qh;
            u32 ah = ao_, sf = ss + 4096 + 64;
            uo = gq ? BIG : lb + (sx[0] >> 2);
            if (ly) {
                wph = L + L_Q + 1024 + hpos(n0 < 12 ? hrow(n0) : 0, gq, 0) * 16;
                ah = BIG;
                uo = ((n0 & 3) * 4 + (n0 >> 2)) * 896 + gq * 8;
                uo2 = gq < 3 ? uo : BIG;
                lw8(L + lsc + n0 * 64 + gq * 8, v2u{bc<u32>(c1[0]), bc<u32>(c1[1])});
                lw4(L + lsc + n0 * 64 + gq * 4 + 32, bc<u32>(c1[2]));
                c1 = bc<f4>(lr16(L + lsc + (gq < 3 ? vo : vo - 48)));
            }
            asm volatile("" : "+v"(wph), "+v"(ah), "+v"(uo), "+v"(uo2));
            asm volatile("" : "+s"(sf));
            sb();
            if (QL == 4) TQ(0);
            u32 it = 0, thr, bad;
            wait_lds2(co, c_ot + 8, want);
#pragma unroll 1
            do {
                if (COLD(it)) ldh_();
                lim(it, thr);
                sb();
                TS(37 + 8 * ly);
#pragma unroll
                for (int j = 0; j < 8; ++j) {
                    bad = j ? xor_or(hb[j].w, gh, bad) : hb[j].w ^ gh;
                    if (j == 0) keep(f0, f1);
                    if (QL == 4 && j == 0) TQ(1);
                    if (j < 4) {
                        hb[j].w = (hb[j].w & 0xffffu) | kpad;
                        lw16(hp + j * 2048, hb[j]);
                    } else {
                        hb[j].w = (hb[j].w & 0xffffu) | 0x3f800000u;
                        lw8(hv + 1024 + (j - 4) * 2048, v2u{hb[j].x, hb[j].y});
                        lw8(hv + 1536 + (j - 4) * 2048, v2u{hb[j].z, hb[j].w});
                    }
                    sb();
                }
            } while (torn(bad, thr));
            TN(49 + ly, it - 1);
            if (QL == 4) TQ(2);
            TS(38 + 8 * ly);
            meet<0>(c_kv, want);
            v4u kf[17];
#pragma unroll
            for (int u = 0; u < 17; ++u) {
                kf[u] = lr16(ak + u * 2048);
                sb();
            }
            prio1<2>(pm);
            sb();
            TS(11 + 8 * ly);
            if (QL == 4) TQ(3);
            f4 t[17];
            v2u vf[17][2];
            float mx;
#pragma unroll
            for (int u = 0; u < 17; ++u) {
                t[u] = mma(kf[u], qf, u == 0 ? mf : u == 16 ? ml : z4);
                vf[u][0] = ltr(av + 1024 + u * 2048);
                vf[u][1] = ltr(av + 1536 + u * 2048);
                if (u == 2) mx = fmaxf(fmaxf(t[0][0], t[0][1]), fmaxf(t[0][2], t[0][3]));
                if (u > 2) mx = fmaxf(fmaxf(fmaxf(fmaxf(mx, t[u - 2][0]), t[u - 2][1]), t[u - 2][2]), t[u - 2][3]);
                sb();
            }
#pragma unroll
            for (int u = 15; u < 17; ++u) mx = fmaxf(fmaxf(fmaxf(fmaxf(mx, t[u][0]), t[u][1]), t[u][2]), t[u][3]);
            sig_all(c_kv + 8);
            prio1<3>(pm);
            if (QL == 4) TQ(4);
            f16 s[4];
#pragma unroll
            for (int i = 0; i < 64; ++i) s[i >> 4][i & 15] = t[i >> 2][i & 3];
            f4 so = t[16];
            mx = rows_max(mx);
            const float r1 = mx - bhi(bc<u32>(mx)), r2 = r1 - bhi(bc<u32>(r1));
            const v4u mp = {__builtin_amdgcn_perm(bc<u32>(r1), bc<u32>(mx), 0x07060302u), bc<u32>(r2) >> 16, 0u, 0u};
            auto sub = [&](int g) { s[g] = __builtin_amdgcn_mfma_f32_32x32x16_bf16(bc<bf8>(m1), bc<bf8>(mp), s[g], 0, 0, 0); };
            sub(0);
            sb();
#pragma unroll
            for (int e = 0; e < 4; ++e) so[e] = __builtin_amdgcn_exp2f(so[e] - mx);
            v4u pf = {pk(so[0], so[1]), pk(so[2], so[3]), 0u, 0u};
            sb();
            sub(1);
            f4 o0, o1;
            u32 cw;
            if (QL == 4) TQ(5);
            sb();
            sfor<9>([&](auto u_) {
                constexpr int u = u_, g = (u >> 1) & 3, e0 = (u & 1) * 8, ua = u ? 2 * u - 2 : 16, ub = u ? 2 * u - 1 : 16;
                auto ex = [&](int e) {
#pragma unroll
                    for (int i = e; i < e + 4; ++i) s[g][i] = __builtin_amdgcn_exp2f(s[g][i]);
                    sb();
                };
                if (u < 8) ex(e0);
                o1 = mma(cat(vf[ua][1], vf[ub][1]), pf, u ? o1 : z4);
                sb();
                if (u < 8) ex(e0 + 4);
                o0 = mma(cat(vf[ua][0], vf[ub][0]), pf, u ? o0 : z4);
                sb();
                if (u < 8) pf = v4u{pk(s[g][e0], s[g][e0 + 1]), pk(s[g][e0 + 2], s[g][e0 + 3]), pk(s[g][e0 + 4], s[g][e0 + 5]), pk(s[g][e0 + 6], s[g][e0 + 7])};
                if (u == 1 || u == 3) sub(g + 2);
                if (u == 7) cw = lrv(cs + (L_CNT - L_CS + 24));
                if (u == HO) {
                    PRIO(3);
                    arrive(tv_, rws, ao_, of_);
                    if (EP) probe_(f0, rws, po, of_);
                }
                sb();
            });
            if (QL == 4) TQ(6);
            const float inv = __builtin_amdgcn_rcpf(o1[3]);
            o0 *= inv;
            o1 *= inv;
            v4u of[16];
            of[0] = v4u{pk(o0[0], o0[1]), pk(o0[2], o0[3]), pk(o1[0], o1[1]), pk(o1[2], bc<float>(gt))};
            st16s<16>(of[0], rws, ol, sx[0]);
            probe_(EP ? f1 : f0, rws, po, of_);
            PRIO(1);
            sb();
            if (QL == 4) TQ(7);
            TS(12 + 8 * ly);
            if (QL == 4) { TF(0, 13); TF(1, 21); TF(2, 29); TF(3, 30); TF(4, 31); TF(5, 32); TF(6, 39); TF(7, 40); }
            wait_lds2(cw, cnt + 24, 4);
            v4u wg[32];
            auto ldg = [&](int j) { wg[j] = lr16(((j & 1) ? wph : ql) + (L_WO - L_Q) + (j >> 1) * QSTEP); };
#pragma unroll
            for (int j = 0; j < WG1; ++j) {
                ldg(j);
                sb();
            }
            if (!EP) probe_(f1, rws, po, of_);
            f4 b0 = mma(wg[0], of[0], c0), b1 = mma(wg[1], of[0], c1);
            PRIO(3);
            if (QL == 3) TQ(0);
            wait2<0, 1 + EP>(f0, f1, rws, po, of_, tag);
            if (QL == 3) TQ(1);
            it = 0;
#pragma unroll 1
            do {
                cbar();
#pragma unroll
                for (int s = 1; s < 16; ++s) {
                    of[s] = ld16s<16>(rws, ol, sx[s]);
                    if (s == 2) PRIO(0);
                    sb();
#pragma unroll
                    for (int j = WG1 + (s - 1) * (32 - WG1) / 15; j < WG1 + s * (32 - WG1) / 15; ++j) ldg(j);
                    sb();
                }
                prio1<4>(pm);
                if (QL != 3) TS(14 + 8 * ly);
                lim(it, thr);
                if (QL == 3) TQ(2);
#pragma unroll
                for (int s = 1; s < 16; ++s) {
                    c0 = mma(wg[2 * s], of[s], s > 1 ? c0 : b0);
                    if (s == 1) keep(f0, f1);
                    bad = s > 1 ? xor_or(of[s].w, gt, bad) : of[s].w ^ gt;
                    c1 = mma(wg[2 * s + 1], of[s], s > 1 ? c1 : b1);
                    if (QL == 3 && s == 1) TQ(3);
                    if (QL == 3 && s == 5) TQ(4);
                    if (QL == 3 && s == 9) TQ(5);
                    if (QL == 3 && s == 12) TQ(6);
                    if (QL == 3 && s == 15) TQ(7);
                    if (s == HS) {
                        PRIO(3);
                        arrive(tv_, rws, ah, sf);
                    }
                    sb();
                }
            } while (torn(bad, thr));
            TN(51 + ly, it - 1);
            TS(33 + 8 * ly);
            if (QL == 3) { TF(0, 13); TF(1, 21); TF(2, 29); TF(3, 30); TF(4, 31); TF(5, 32); TF(6, 39); TF(7, 40); }
            if (ly) {
                const rsrc_t ro = mkrs(out, SD2);
                st8s<0>(v2u{pk(c0[0], c0[1]), pk(c0[2], c0[3])}, ro, uo, us);
                st8s<0>(v2u{pk(c1[0], c1[1]), pk(c1[2], c1[3])}, ro, uo2 + 32, us);
                TS(23);
        TC(63);
                asm volatile("s_endpgm" ::: "memory");
            }
            const float s1 = rows_sum(((c0[0] + c0[1]) + (c0[2] + c0[3])) + ((c1[0] + c1[1]) + c1[2]));
            const float s2 = rows_sum(fm(c0[0], c0[0], fm(c0[1], c0[1], fm(c0[2], c0[2], c0[3] * c0[3]))) + fm(c1[0], c1[0], fm(c1[1], c1[1], c1[2] * c1[2])));
            st16s<16>(v4u{bc<u32>(s1), bc<u32>(s2), gt, gt}, rws, uo, ss);
            probe_(f0, rws, po, sf);
            PRIO(1);
            sb();
            TS(34);
            u32 xf_ = ss + 4096 + 128, xl = lb + (lb < 896 ? xs : xt);
            kvo += 512 * KP, as += 128, hf_ += 128, hs += 512 * KP, want = 8, pm >>= 8;
            const u32 g0 = gt;
            gt ^= 0x80000000u, gh = kpad ? 0u : gt;
            asm volatile("" : "+v"(xl));
            asm volatile("" : "+s"(xf_), "+s"(kvo), "+s"(as), "+s"(hs), "+s"(hf_), "+s"(want), "+s"(gt), "+s"(gh), "+s"(pm));
            yo = v4u{dpm<0x118, 0xc>(yo.x, yo.z), dpm<0x118, 0xc>(yo.y, yo.w), dpm<0x108, 0x3>(yo.z, yo.x), dpm<0x108, 0x3>(yo.w, yo.y)};
            yo = v4u{dpm<0x114, 0xa>(yo.x, yo.y), dpm<0x104, 0x5>(yo.y, yo.x), dpm<0x114, 0xa>(yo.z, yo.w), dpm<0x104, 0x5>(yo.w, yo.z) >> (vo & 16)};
            f4 y0 = {blo(yo.x), bhi(yo.x), blo(yo.y), bhi(yo.y)}, y1 = {blo(yo.z), bhi(yo.z), blo(yo.w), 0.f};
            f4 z0 = c0 * y0, z1 = c1 * y1;
            asm volatile("" : "+v"(y0), "+v"(y1), "+v"(z0), "+v"(z1));
            __builtin_amdgcn_s_sleep(SS);
            PRIO(3);
            probe_(f1, rws, po, sf);
            wait2<0>(f0, f1, rws, po, sf, tag);
            v4u sp[4];
            it = 0;
#pragma unroll 1
            do {
                cbar();
#pragma unroll
                for (int m = 0; m < 4; ++m) sp[m] = ld16s<16>(rws, lb + m * 1024, ss);
                sb();
                lim(it, thr);
                TS(15);
                if (HX) {
                    __builtin_amdgcn_s_sleep(HX - 1);
                    arrive(tv_, rws, ao_, xf_);
                    sb();
                }
            } while (torn(xor_or(sp[3].w, g0, xor_or(sp[2].w, g0, xor_or(sp[1].w, g0, sp[0].w ^ g0))), thr));
            TN(53, it - 1);
            keep(f0, f1);
            if (!HX) arrive(tv_, rws, ao_, xf_);
            sb();
            const float n1 = rows_sum((bc<float>(sp[0].x) + bc<float>(sp[1].x)) + (bc<float>(sp[2].x) + bc<float>(sp[3].x))) * (-1.f / 448);
            const float m2 = rows_sum((bc<float>(sp[0].y) + bc<float>(sp[1].y)) + (bc<float>(sp[2].y) + bc<float>(sp[3].y))) * (1.f / 448);
            const float xr = __builtin_amdgcn_rsqf(fmaxf(fm(-n1, n1, m2), 0.f) + 1e-5f);
#pragma unroll
            for (int e = 0; e < 4; ++e) z0[e] = pv(fm(n1, y0[e], z0[e]));
#pragma unroll
            for (int e = 0; e < 3; ++e) z1[e] = pv(fm(n1, y1[e], z1[e]));
#pragma unroll
            for (int e = 0; e < 4; ++e) z0[e] = pv(fm(z0[e], xr, c0[e]));
#pragma unroll
            for (int e = 0; e < 3; ++e) z1[e] = pv(fm(z1[e], xr, c1[e]));
            c0 = z0, c1 = z1;
            const v4u x3 = {pk(c0[0], c0[1]), pk(c0[2], c0[3]), pk(c1[0], c1[1]), pk(c1[2], bc<float>(gt))};
            st16s<16>(x3, rws, xl, sx[0]);
            kvproj(std::false_type{}, xl, x3, xf_);
            ly = 1;
            asm volatile("" : "+s"(ly));
        }
    } while (ly);
}
}

extern "C" void kernel_launch(void* const* d_in, const int* in_sizes, int n_in, void* d_out, int out_size,
                              void* d_ws, size_t ws_size, hipStream_t stream) {
    (void)in_sizes; (void)n_in; (void)out_size; (void)ws_size;
    (void)hipFuncSetAttribute((const void*)k_main, hipFuncAttributeMaxDynamicSharedMemorySize, (int)L_END);
    hipLaunchKernelGGL(k_main, dim3(256), dim3(512), L_END, stream, (const u16*)d_in[0], (const u16*)d_in[1],
                       (const u16*)d_in[2], (const u16*)d_in[3], (const u16*)d_in[4], (const u16*)d_in[5],
                       (const u16*)d_in[7], (const u16*)d_in[8], (u16*)d_out, (char*)d_ws);
}
```
